```python
import torch

def _softmax(x, axis=-1):
    return torch.softmax(x, dim=axis)
def _gelu(x):
    return torch.nn.functional.gelu(x, approximate="tanh")
def _silu(x):
    return torch.nn.functional.silu(x)
def _sigmoid(x):
    return torch.sigmoid(x)
def _layernorm(x, axis=-1, eps=1e-5):
    m = x.mean(dim=axis, keepdim=True)
    v = x.var(dim=axis, keepdim=True, unbiased=False)
    return (x - m) / torch.sqrt(v + eps)
def _scatter_add(tgt, idx, src, axis=0):
    return tgt.clone().index_add_(axis, idx, src)
def _segment_sum(src, seg, n):
    out = torch.zeros((n,) + src.shape[1:], dtype=src.dtype)
    return out.index_add_(0, seg, src)
def _pad(x, pw):
    flat = tuple(v for pair in reversed(pw) for v in pair)
    return torch.nn.functional.pad(x, flat)

kernel_name = 'concat_heads__mha_block_cached__mha_block_cached__near_ridge'

def setup_inputs():

    _w = (lambda a: (lambda t: t.to(torch.bfloat16) if t.is_floating_point() else t)(torch.as_tensor(a)))
    def _A_setup():
        B, Sq, D = 16, 32, 1024
        H, Sk, Dh = 32, 32, 128
        return {
            "x": _w((lambda *s: torch.randn(*s, dtype=torch.bfloat16) * 0.8)(B, Sq, D)),
            "k_cache": _w((lambda *s: torch.randn(*s, dtype=torch.bfloat16) * 0.8)(B, H, Sk, Dh)),
            "v_cache": _w((lambda *s: torch.randn(*s, dtype=torch.bfloat16) * 0.05)(B, H, Sk, Dh)),
            "wq": _w((lambda *s: torch.randn(*s, dtype=torch.bfloat16) * 0.8)(D, H * Dh) * (D ** -0.5)),
            "wo": _w((lambda *s: torch.randn(*s, dtype=torch.bfloat16) * 0.05)(H * Dh, D) * ((H * Dh) ** -0.5)),
        }
    _A_in = _A_setup()

    def _B_setup():
        B, Sq, D = 16, 32, 1024
        H, Sk, Dh = 32, 32, 128
        return {
            "x": _w((lambda *s: torch.randn(*s, dtype=torch.bfloat16) * 0.8)(B, Sq, D)),
            "k_cache": _w((lambda *s: torch.randn(*s, dtype=torch.bfloat16) * 0.8)(B, H, Sk, Dh)),
            "v_cache": _w((lambda *s: torch.randn(*s, dtype=torch.bfloat16) * 0.05)(B, H, Sk, Dh)),
            "wq": _w((lambda *s: torch.randn(*s, dtype=torch.bfloat16) * 0.8)(D, H * Dh) * (D ** -0.5)),
            "wo": _w((lambda *s: torch.randn(*s, dtype=torch.bfloat16) * 0.05)(H * Dh, D) * ((H * Dh) ** -0.5)),
        }
    _B_in = _B_setup()

    return {
        "A_x": _A_in["x"],
        "A_k_cache": _A_in["k_cache"],
        "A_v_cache": _A_in["v_cache"],
        "A_wq": _A_in["wq"],
        "A_wo": _A_in["wo"],
        "B_x": _B_in["x"],
        "B_k_cache": _B_in["k_cache"],
        "B_v_cache": _B_in["v_cache"],
        "B_wq": _B_in["wq"],
        "B_wo": _B_in["wo"],
    }

def reference(A_x, A_k_cache, A_v_cache, A_wq, A_wo, B_x, B_k_cache, B_v_cache, B_wq, B_wo):
    def _A_ref(x, k_cache, v_cache, wq, wo):
        B, Sq, D = x.shape
        H, Dh = 32, 128
        q = (lambda x, *d: x.permute(*d))((x @ wq).reshape(B, Sq, H, Dh), 0, 2, 1, 3)
        scores = torch.einsum("bhqd,bhkd->bhqk", q, k_cache) * (Dh ** -0.5)
        p = _softmax(scores, axis=-1)
        o = torch.einsum("bhqk,bhkd->bhqd", p, v_cache)
        return (lambda x, *d: x.permute(*d))(o, 0, 2, 1, 3).reshape(B, Sq, H * Dh) @ wo

    def _B_ref(x, k_cache, v_cache, wq, wo):
        B, Sq, D = x.shape
        H, Dh = 32, 128
        q = (lambda x, *d: x.permute(*d))((x @ wq).reshape(B, Sq, H, Dh), 0, 2, 1, 3)
        scores = torch.einsum("bhqd,bhkd->bhqk", q, k_cache) * (Dh ** -0.5)
        p = _softmax(scores, axis=-1)
        o = torch.einsum("bhqk,bhkd->bhqd", p, v_cache)
        return (lambda x, *d: x.permute(*d))(o, 0, 2, 1, 3).reshape(B, Sq, H * Dh) @ wo

    a = _A_ref(A_x, A_k_cache, A_v_cache, A_wq, A_wo)
    b = _B_ref(B_x, B_k_cache, B_v_cache, B_wq, B_wo)
    return torch.concatenate([a, b], axis=-1)
```

```cpp
#include <hip/hip_runtime.h>
#include <hip/hip_bf16.h>
#include <stdint.h>

#define DI __device__ __forceinline__
#define LAS __attribute__((address_space(3)))
#define GAS __attribute__((address_space(1)))
typedef float f32x16 __attribute__((ext_vector_type(16)));
typedef float f32x4 __attribute__((ext_vector_type(4)));
typedef float f32x2 __attribute__((ext_vector_type(2)));
typedef short s16x4 __attribute__((ext_vector_type(4)));
typedef short s16x8 __attribute__((ext_vector_type(8)));
typedef __bf16 bf16x8 __attribute__((ext_vector_type(8)));
typedef __bf16 bf16x2 __attribute__((ext_vector_type(2)));
typedef unsigned u32x4 __attribute__((ext_vector_type(4)));
typedef unsigned u32x2 __attribute__((ext_vector_type(2)));

constexpr int NB = 16, SQ = 32, DM = 1024, NH = 32, SK = 32, DH = 128, HD = NH * DH;

DI u32x4 make_rsrc(const void* p, unsigned bytes) {
  uint64_t a = (uint64_t)p;
  u32x4 r;
  r.x = __builtin_amdgcn_readfirstlane((unsigned)a);
  r.y = __builtin_amdgcn_readfirstlane((unsigned)(a >> 32)) & 0xffffu;
  r.z = bytes;
  r.w = 0x00020000u;
  return r;
}
DI void dma16(unsigned lds, unsigned voff, u32x4 rsrc, unsigned soff) {
  asm volatile("s_mov_b32 m0, %0\n\ts_nop 0\n\tbuffer_load_dwordx4 %1, %2, %3 offen lds"
               :: "s"(lds), "v"(voff), "s"(rsrc), "s"(soff) : "memory");
}
#define WAITV(n) asm volatile("s_waitcnt vmcnt(" #n ")" ::: "memory")
#define BARRIER() asm volatile("s_waitcnt lgkmcnt(0)\n\ts_barrier" ::: "memory")
DI f32x16 mfma(u32x4 a, u32x4 b, f32x16 c) {
  return __builtin_amdgcn_mfma_f32_32x32x16_bf16(__builtin_bit_cast(bf16x8, a), __builtin_bit_cast(bf16x8, b), c, 0, 0, 0);
}
DI u32x2 trread(LAS char* p) { return __builtin_bit_cast(u32x2, __builtin_amdgcn_ds_read_tr16_b64_v4i16((LAS s16x4*)p)); }
DI u32x4 trread2(LAS char* p0, LAS char* p1) { u32x2 a = trread(p0), b = trread(p1); return u32x4{a.x, a.y, b.x, b.y}; }
DI unsigned cvtpk(float lo, float hi) { f32x2 v = {lo, hi}; return __builtin_bit_cast(unsigned, __builtin_convertvector(v, bf16x2)); }
DI u32x4 pack8(const f32x16& x, int s) {
  return u32x4{cvtpk(x[8 * s], x[8 * s + 1]), cvtpk(x[8 * s + 2], x[8 * s + 3]), cvtpk(x[8 * s + 4], x[8 * s + 5]), cvtpk(x[8 * s + 6], x[8 * s + 7])};
}

struct Params {
  const void* x[2]; const void* k[2]; const void* v[2]; const void* wq[2]; const void* wo[2];
  void* out; void* ws;
};

constexpr int K1_STAGE = 32768, K1_NS = 4, K1_VOFF = K1_STAGE * K1_NS, K1_LDS = K1_VOFF + 32768;

__global__ void __launch_bounds__(256, 1) k1_qattn(Params p) {
  extern __shared__ __attribute__((aligned(16))) char smem_[];
  LAS char* smem = (LAS char*)smem_;
  const unsigned sbase = (unsigned)(uintptr_t)smem;
  const int bid = blockIdx.x, xcd = bid & 7, i = bid >> 3;
  const int br = xcd >> 2, h = (xcd & 3) * 8 + (i & 7), rg = i >> 3;
  const int tid = threadIdx.x, lane = tid & 63, w = __builtin_amdgcn_readfirstlane(tid >> 6);
  const int b = rg * 4 + w;
  const u32x4 rx = make_rsrc(p.x[br], NB * SQ * DM * 2), rw = make_rsrc(p.wq[br], DM * HD * 2);
  const u32x4 rv = make_rsrc(p.v[br], NB * NH * SK * DH * 2);
  unsigned vx[4], vw[4];
#pragma unroll
  for (int j = 0; j < 4; ++j) {
    int r = 8 * j + (lane >> 3);
    vx[j] = (b * SQ + r) * (DM * 2) + (((lane & 7) ^ ((r >> 1) & 7)) << 4);
    int kr = 16 * w + 4 * j + (lane >> 4);
    vw[j] = kr * (HD * 2) + h * (DH * 2) + (((lane & 15) ^ ((kr & 3) << 2)) << 4);
  }
  auto stage = [&](int t) {
    const unsigned l = sbase + (t & (K1_NS - 1)) * K1_STAGE;
#pragma unroll
    for (int j = 0; j < 4; ++j) dma16(l + w * 4096 + j * 1024, vx[j], rx, t * 128);
#pragma unroll
    for (int j = 0; j < 4; ++j) dma16(l + 16384 + w * 4096 + j * 1024, vw[j], rw, t * 64 * (HD * 2));
  };
  stage(0); stage(1); stage(2);
  const int q = lane & 31, hh = lane >> 5;
  const int tq = (lane & 15) >> 2, tp = lane & 3, tblk = (lane >> 4) & 1;
  const int tps = ((tp & 1) << 1) | (tp >> 1);
  unsigned xa[4];
#pragma unroll
  for (int ks = 0; ks < 4; ++ks) xa[ks] = w * 4096 + q * 128 + (((2 * ks + hh) ^ ((q >> 1) & 7)) << 4);
  unsigned wa[4];
#pragma unroll
  for (int dt = 0; dt < 4; ++dt) wa[dt] = 16384 + (8 * hh + tq) * 256 + ((dt ^ tq) << 6) + tblk * 32 + tps * 8;
  f32x16 acc[4] = {};
  for (int t = 0; t < 16; ++t) {
    if (t < 14) WAITV(16); else if (t == 14) WAITV(16); else WAITV(8);
    BARRIER();
    if (t + 3 < 16) stage(t + 3);
    else if (t == 13) {
#pragma unroll
      for (int j = 0; j < 8; ++j) {
        int key = 4 * j + (lane >> 4);
        dma16(sbase + K1_VOFF + w * 8192 + j * 1024, lane * 0 + (key * 256 + (((lane & 15) ^ ((key & 3) << 2)) << 4)), rv, (b * NH + h) * (SK * DH * 2));
      }
    }
    LAS char* l = smem + (t & (K1_NS - 1)) * K1_STAGE;
#pragma unroll
    for (int ks = 0; ks < 4; ++ks) {
      u32x4 xf = *(LAS u32x4*)(l + xa[ks]);
#pragma unroll
      for (int dt = 0; dt < 4; ++dt) {
        u32x4 wf = trread2(l + wa[dt] + ks * 4096, l + wa[dt] + ks * 4096 + 1024);
        acc[dt] = mfma(wf, xf, acc[dt]);
      }
    }
  }
  const GAS u32x4* kp = (const GAS u32x4*)((const char*)p.k[br] + (size_t)(b * NH + h) * (SK * DH * 2) + q * 256 + hh * 16);
  f32x16 s = {};
#pragma unroll
  for (int dt = 0; dt < 4; ++dt)
#pragma unroll
    for (int ss = 0; ss < 2; ++ss) s = mfma(kp[(dt * 2 + ss) * 2], pack8(acc[dt], ss), s);
  float m = s[0];
#pragma unroll
  for (int r = 1; r < 16; ++r) m = fmaxf(m, s[r]);
  m = fmaxf(m, __shfl_xor(m, 32));
  const float c = 0.08838834764831845f * 1.4426950408889634f;
  float sum = 0.f;
#pragma unroll
  for (int r = 0; r < 16; ++r) { s[r] = __builtin_amdgcn_exp2f((s[r] - m) * c); sum += s[r]; }
  sum += __shfl_xor(sum, 32);
  const float inv = 1.0f / sum;
#pragma unroll
  for (int r = 0; r < 16; ++r) s[r] *= inv;
  const u32x4 p0 = pack8(s, 0), p1 = pack8(s, 1);
  WAITV(0);
  LAS char* vl = smem + K1_VOFF + w * 8192 + (4 * hh + tq) * 256 + tblk * 32 + tps * 8;
  GAS u32x4* op = (GAS u32x4*)((char*)p.ws + ((size_t)(br * NB + b) * 256 + h * 8) * 1024 + lane * 16);
#pragma unroll
  for (int dt = 0; dt < 4; ++dt) {
    LAS char* a = vl + ((dt ^ tq) << 6);
    f32x16 o = {};
    o = mfma(trread2(a, a + 8 * 256), p0, o);
    o = mfma(trread2(a + 16 * 256, a + 24 * 256), p1, o);
    op[(dt * 2) * 64] = pack8(o, 0);
    op[(dt * 2 + 1) * 64] = pack8(o, 1);
  }
}

constexpr int K2_STAGE = 8192, K2_NS = 4, K2_LDS = 4 * K2_NS * K2_STAGE;
__global__ void __launch_bounds__(256, 1) k2_oproj(Params p) {
  extern __shared__ __attribute__((aligned(16))) char smem_[];
  LAS char* smem = (LAS char*)smem_;
  const unsigned sbase = (unsigned)(uintptr_t)smem;
  const int bid = blockIdx.x, xcd = bid & 7, i = bid >> 3;
  const int br = xcd >> 2, n0 = (xcd & 3) * 256 + (i & 3) * 64, rt = i >> 2;
  const int tid = threadIdx.x, lane = tid & 63, w = __builtin_amdgcn_readfirstlane(tid >> 6);
  const u32x4 rw = make_rsrc(p.wo[br], HD * DM * 2);
  const int kr = lane >> 3;
  const unsigned vw = (w * 1024 + kr) * (DM * 2) + n0 * 2 + (((lane & 7) ^ (((kr >> 1) & 1) << 2)) << 4);
  auto stage = [&](int t) {
    const unsigned l = sbase + (w * K2_NS + (t & (K2_NS - 1))) * K2_STAGE;
#pragma unroll
    for (int j = 0; j < 8; ++j) dma16(l + j * 1024, vw, rw, (t * 64 + j * 8) * (DM * 2));
  };
  const GAS u32x4* op = (const GAS u32x4*)((const char*)p.ws + ((size_t)(br * NB + rt * 2) * 256 + w * 64) * 1024 + lane * 16);
  const int hh = lane >> 5, tq = (lane & 15) >> 2, tp = lane & 3, tblk = (lane >> 4) & 1;
  const int tps = ((tp & 1) << 1) | (tp >> 1);
  unsigned wa[2];
#pragma unroll
  for (int nt = 0; nt < 2; ++nt) wa[nt] = (8 * hh + tq) * 128 + ((nt ^ (tq >> 1)) << 6) + tblk * 32 + tps * 8;
  f32x16 acc[2][2] = {};
  u32x4 of[3][8];
  auto loado = [&](int t, u32x4 (&d)[8]) {
#pragma unroll
    for (int ks = 0; ks < 4; ++ks) {
      asm volatile("global_load_dwordx4 %0, %1, off" : "=v"(d[2 * ks]) : "v"(op + (t * 4 + ks) * 64) : "memory");
      asm volatile("global_load_dwordx4 %0, %1, off" : "=v"(d[2 * ks + 1]) : "v"(op + (256 + t * 4 + ks) * 64) : "memory");
    }
  };
  auto comp = [&](int t, u32x4 (&d)[8]) {
    asm volatile("" : "+v"(d[0]), "+v"(d[1]), "+v"(d[2]), "+v"(d[3]), "+v"(d[4]), "+v"(d[5]), "+v"(d[6]), "+v"(d[7]));
    LAS char* l = smem + (w * K2_NS + (t & (K2_NS - 1))) * K2_STAGE;
#pragma unroll
    for (int ks = 0; ks < 4; ++ks)
#pragma unroll
      for (int nt = 0; nt < 2; ++nt) {
        u32x4 wf = trread2(l + wa[nt] + ks * 2048, l + wa[nt] + ks * 2048 + 512);
        acc[nt][0] = mfma(wf, d[2 * ks], acc[nt][0]);
        acc[nt][1] = mfma(wf, d[2 * ks + 1], acc[nt][1]);
      }
    asm volatile("s_waitcnt lgkmcnt(0)" ::: "memory");
  };
  stage(0); loado(0, of[0]); stage(1); loado(1, of[1]);
#pragma unroll
  for (int t = 0; t < 16; ++t) {
    if (t + 2 < 16) { stage(t + 2); loado(t + 2, of[(t + 2) % 3]); WAITV(32); }
    else if (t == 14) WAITV(16); else WAITV(0);
    comp(t, of[t % 3]);
  }
  BARRIER();
#pragma unroll
  for (int nt = 0; nt < 2; ++nt)
#pragma unroll
    for (int qt = 0; qt < 2; ++qt)
#pragma unroll
      for (int g = 0; g < 4; ++g)
        *(LAS f32x4*)(smem + ((w * 4 + nt * 2 + qt) * 4 + g) * 1024 + lane * 16) =
            f32x4{acc[nt][qt][4 * g], acc[nt][qt][4 * g + 1], acc[nt][qt][4 * g + 2], acc[nt][qt][4 * g + 3]};
  BARRIER();
  f32x16 r;
#pragma unroll
  for (int g = 0; g < 4; ++g) {
    f32x4 a = {};
#pragma unroll
    for (int ww = 0; ww < 4; ++ww) a += *(LAS f32x4*)(smem + ((ww * 4 + w) * 4 + g) * 1024 + lane * 16);
    r[4 * g] = a.x; r[4 * g + 1] = a.y; r[4 * g + 2] = a.z; r[4 * g + 3] = a.w;
  }
  const int row = rt * 64 + (w & 1) * 32 + (lane & 31);
  GAS u32x4* dst = (GAS u32x4*)((char*)p.out + (size_t)row * (2 * DM * 2) + (br * DM + n0 + (w >> 1) * 32 + hh * 8) * 2);
  dst[0] = pack8(r, 0);
  dst[2] = pack8(r, 1);
}

extern "C" void kernel_launch(void* const* d_in, const int* in_sizes, int n_in, void* d_out, int out_size, void* d_ws,
                              size_t ws_size, hipStream_t stream) {
  Params p;
  p.x[0] = d_in[0]; p.k[0] = d_in[1]; p.v[0] = d_in[2]; p.wq[0] = d_in[3]; p.wo[0] = d_in[4];
  p.x[1] = d_in[5]; p.k[1] = d_in[6]; p.v[1] = d_in[7]; p.wq[1] = d_in[8]; p.wo[1] = d_in[9];
  p.out = d_out; p.ws = d_ws;
  hipFuncSetAttribute((const void*)k1_qattn, hipFuncAttributeMaxDynamicSharedMemorySize, K1_LDS);
  hipFuncSetAttribute((const void*)k2_oproj, hipFuncAttributeMaxDynamicSharedMemorySize, K2_LDS);
  k1_qattn<<<256, 256, K1_LDS, stream>>>(p);
  k2_oproj<<<256, 256, K2_LDS, stream>>>(p);
}
```

```cpp
#include <hip/hip_runtime.h>
#include <hip/hip_bf16.h>
#include <stdint.h>

#define DI __device__ __forceinline__
#define LAS __attribute__((address_space(3)))
#define GAS __attribute__((address_space(1)))
typedef float f32x16 __attribute__((ext_vector_type(16)));
typedef float f32x4 __attribute__((ext_vector_type(4)));
typedef float f32x2 __attribute__((ext_vector_type(2)));
typedef short s16x4 __attribute__((ext_vector_type(4)));
typedef short s16x8 __attribute__((ext_vector_type(8)));
typedef __bf16 bf16x8 __attribute__((ext_vector_type(8)));
typedef __bf16 bf16x2 __attribute__((ext_vector_type(2)));
typedef unsigned u32x4 __attribute__((ext_vector_type(4)));
typedef unsigned u32x2 __attribute__((ext_vector_type(2)));

constexpr int NB = 16, SQ = 32, DM = 1024, NH = 32, SK = 32, DH = 128, HD = NH * DH;

DI u32x4 make_rsrc(const void* p, unsigned bytes) {
  uint64_t a = (uint64_t)p;
  u32x4 r;
  r.x = __builtin_amdgcn_readfirstlane((unsigned)a);
  r.y = __builtin_amdgcn_readfirstlane((unsigned)(a >> 32)) & 0xffffu;
  r.z = bytes;
  r.w = 0x00020000u;
  return r;
}
DI void dma16(unsigned lds, unsigned voff, u32x4 rsrc, unsigned soff) {
  asm volatile("s_mov_b32 m0, %0\n\ts_nop 0\n\tbuffer_load_dwordx4 %1, %2, %3 offen lds"
               :: "s"(lds), "v"(voff), "s"(rsrc), "s"(soff) : "memory");
}
#define WAITV(n) asm volatile("s_waitcnt vmcnt(" #n ")" ::: "memory")
#define BARRIER() asm volatile("s_waitcnt lgkmcnt(0)\n\ts_barrier" ::: "memory")
DI f32x16 mfma(u32x4 a, u32x4 b, f32x16 c) {
  return __builtin_amdgcn_mfma_f32_32x32x16_bf16(__builtin_bit_cast(bf16x8, a), __builtin_bit_cast(bf16x8, b), c, 0, 0, 0);
}
DI u32x2 trread(LAS char* p) { return __builtin_bit_cast(u32x2, __builtin_amdgcn_ds_read_tr16_b64_v4i16((LAS s16x4*)p)); }
DI u32x4 trread2(LAS char* p0, LAS char* p1) { u32x2 a = trread(p0), b = trread(p1); return u32x4{a.x, a.y, b.x, b.y}; }
DI unsigned cvtpk(float lo, float hi) { f32x2 v = {lo, hi}; return __builtin_bit_cast(unsigned, __builtin_convertvector(v, bf16x2)); }
DI u32x4 pack8(const f32x16& x, int s) {
  return u32x4{cvtpk(x[8 * s], x[8 * s + 1]), cvtpk(x[8 * s + 2], x[8 * s + 3]), cvtpk(x[8 * s + 4], x[8 * s + 5]), cvtpk(x[8 * s + 6], x[8 * s + 7])};
}

struct Params {
  const void* x[2]; const void* k[2]; const void* v[2]; const void* wq[2]; const void* wo[2];
  void* out; void* ws;
};

constexpr int K1_STAGE = 32768, K1_NS = 4, K1_VOFF = K1_STAGE * K1_NS, K1_LDS = K1_VOFF + 32768;

__global__ void __launch_bounds__(256, 1) k1_qattn(Params p) {
  extern __shared__ __attribute__((aligned(16))) char smem_[];
  LAS char* smem = (LAS char*)smem_;
  const unsigned sbase = (unsigned)(uintptr_t)smem;
  const int bid = blockIdx.x, xcd = bid & 7, i = bid >> 3;
  const int br = xcd >> 2, h = (xcd & 3) * 8 + (i & 7), rg = i >> 3;
  const int tid = threadIdx.x, lane = tid & 63, w = __builtin_amdgcn_readfirstlane(tid >> 6);
  const int b = rg * 4 + w;
  const u32x4 rx = make_rsrc(p.x[br], NB * SQ * DM * 2), rw = make_rsrc(p.wq[br], DM * HD * 2);
  const u32x4 rv = make_rsrc(p.v[br], NB * NH * SK * DH * 2);
  unsigned vx[4], vw[4];
#pragma unroll
  for (int j = 0; j < 4; ++j) {
    int r = 8 * j + (lane >> 3);
    vx[j] = (b * SQ + r) * (DM * 2) + (((lane & 7) ^ ((r >> 1) & 7)) << 4);
    int kr = 16 * w + 4 * j + (lane >> 4);
    vw[j] = kr * (HD * 2) + h * (DH * 2) + (((lane & 15) ^ ((kr & 3) << 2)) << 4);
  }
  auto stage = [&](int t) {
    const unsigned l = sbase + (t & (K1_NS - 1)) * K1_STAGE;
#pragma unroll
    for (int j = 0; j < 4; ++j) dma16(l + w * 4096 + j * 1024, vx[j], rx, t * 128);
#pragma unroll
    for (int j = 0; j < 4; ++j) dma16(l + 16384 + w * 4096 + j * 1024, vw[j], rw, t * 64 * (HD * 2));
  };
  stage(0); stage(1); stage(2);
  const int q = lane & 31, hh = lane >> 5;
  const int tq = (lane & 15) >> 2, tp = lane & 3, tblk = (lane >> 4) & 1;
  const int tps = ((tp & 1) << 1) | (tp >> 1);
  unsigned xa[4];
#pragma unroll
  for (int ks = 0; ks < 4; ++ks) xa[ks] = w * 4096 + q * 128 + (((2 * ks + hh) ^ ((q >> 1) & 7)) << 4);
  unsigned wa[4];
#pragma unroll
  for (int dt = 0; dt < 4; ++dt) wa[dt] = 16384 + (8 * hh + tq) * 256 + ((dt ^ tq) << 6) + tblk * 32 + tps * 8;
  f32x16 acc[4] = {};
  for (int t = 0; t < 16; ++t) {
    if (t < 14) WAITV(16); else if (t == 14) WAITV(16); else WAITV(8);
    BARRIER();
    if (t + 3 < 16) stage(t + 3);
    else if (t == 13) {
#pragma unroll
      for (int j = 0; j < 8; ++j) {
        int key = 4 * j + (lane >> 4);
        dma16(sbase + K1_VOFF + w * 8192 + j * 1024, lane * 0 + (key * 256 + (((lane & 15) ^ ((key & 3) << 2)) << 4)), rv, (b * NH + h) * (SK * DH * 2));
      }
    }
    LAS char* l = smem + (t & (K1_NS - 1)) * K1_STAGE;
#pragma unroll
    for (int ks = 0; ks < 4; ++ks) {
      u32x4 xf = *(LAS u32x4*)(l + xa[ks]);
#pragma unroll
      for (int dt = 0; dt < 4; ++dt) {
        u32x4 wf = trread2(l + wa[dt] + ks * 4096, l + wa[dt] + ks * 4096 + 1024);
        acc[dt] = mfma(wf, xf, acc[dt]);
      }
    }
  }
  const GAS u32x4* kp = (const GAS u32x4*)((const char*)p.k[br] + (size_t)(b * NH + h) * (SK * DH * 2) + q * 256 + hh * 16);
  f32x16 s = {};
#pragma unroll
  for (int dt = 0; dt < 4; ++dt)
#pragma unroll
    for (int ss = 0; ss < 2; ++ss) s = mfma(kp[(dt * 2 + ss) * 2], pack8(acc[dt], ss), s);
  float m = s[0];
#pragma unroll
  for (int r = 1; r < 16; ++r) m = fmaxf(m, s[r]);
  m = fmaxf(m, __shfl_xor(m, 32));
  const float c = 0.08838834764831845f * 1.4426950408889634f;
  float sum = 0.f;
#pragma unroll
  for (int r = 0; r < 16; ++r) { s[r] = __builtin_amdgcn_exp2f((s[r] - m) * c); sum += s[r]; }
  sum += __shfl_xor(sum, 32);
  const float inv = 1.0f / sum;
#pragma unroll
  for (int r = 0; r < 16; ++r) s[r] *= inv;
  const u32x4 p0 = pack8(s, 0), p1 = pack8(s, 1);
  WAITV(0);
  LAS char* vl = smem + K1_VOFF + w * 8192 + (4 * hh + tq) * 256 + tblk * 32 + tps * 8;
  GAS u32x4* op = (GAS u32x4*)((char*)p.ws + ((size_t)(br * NB + b) * 256 + h * 8) * 1024 + lane * 16);
#pragma unroll
  for (int dt = 0; dt < 4; ++dt) {
    LAS char* a = vl + ((dt ^ tq) << 6);
    f32x16 o = {};
    o = mfma(trread2(a, a + 8 * 256), p0, o);
    o = mfma(trread2(a + 16 * 256, a + 24 * 256), p1, o);
    op[(dt * 2) * 64] = pack8(o, 0);
    op[(dt * 2 + 1) * 64] = pack8(o, 1);
  }
}

constexpr int K2_STAGE = 8192, K2_NS = 4, K2_LDS = 4 * K2_NS * K2_STAGE;
__global__ void __launch_bounds__(256, 1) k2_oproj(Params p) {
  extern __shared__ __attribute__((aligned(16))) char smem_[];
  LAS char* smem = (LAS char*)smem_;
  const unsigned sbase = (unsigned)(uintptr_t)smem;
  const int bid = blockIdx.x, xcd = bid & 7, i = bid >> 3;
  const int br = xcd >> 2, n0 = (xcd & 3) * 256 + (i & 3) * 64, rt = i >> 2;
  const int tid = threadIdx.x, lane = tid & 63, w = __builtin_amdgcn_readfirstlane(tid >> 6);
  const u32x4 rw = make_rsrc(p.wo[br], HD * DM * 2);
  const int kr = lane >> 3;
  const unsigned vw = (w * 1024 + kr) * (DM * 2) + n0 * 2 + (((lane & 7) ^ (((kr >> 1) & 1) << 2)) << 4);
  auto stage = [&](int t) {
    const unsigned l = sbase + (w * K2_NS + (t & (K2_NS - 1))) * K2_STAGE;
#pragma unroll
    for (int j = 0; j < 8; ++j) dma16(l + j * 1024, vw, rw, (t * 64 + j * 8) * (DM * 2));
  };
  const GAS u32x4* op = (const GAS u32x4*)((const char*)p.ws + ((size_t)(br * NB + rt * 2) * 256 + w * 64) * 1024 + lane * 16);
  const int hh = lane >> 5, tq = (lane & 15) >> 2, tp = lane & 3, tblk = (lane >> 4) & 1;
  const int tps = ((tp & 1) << 1) | (tp >> 1);
  unsigned wa[2];
#pragma unroll
  for (int nt = 0; nt < 2; ++nt) wa[nt] = (8 * hh + tq) * 128 + ((nt ^ (tq >> 1)) << 6) + tblk * 32 + tps * 8;
  f32x16 acc[2][2] = {};
  u32x4 of[3][8];
  auto loado = [&](int t, u32x4 (&d)[8]) {
#pragma unroll
    for (int ks = 0; ks < 4; ++ks) {
      asm volatile("global_load_dwordx4 %0, %1, off" : "=v"(d[2 * ks]) : "v"(op + (t * 4 + ks) * 64) : "memory");
      asm volatile("global_load_dwordx4 %0, %1, off" : "=v"(d[2 * ks + 1]) : "v"(op + (256 + t * 4 + ks) * 64) : "memory");
    }
  };
  auto comp = [&](int t, u32x4 (&d)[8]) {
    asm volatile("" : "+v"(d[0]), "+v"(d[1]), "+v"(d[2]), "+v"(d[3]), "+v"(d[4]), "+v"(d[5]), "+v"(d[6]), "+v"(d[7]));
    LAS char* l = smem + (w * K2_NS + (t & (K2_NS - 1))) * K2_STAGE;
#pragma unroll
    for (int ks = 0; ks < 4; ++ks)
#pragma unroll
      for (int nt = 0; nt < 2; ++nt) {
        u32x4 wf = trread2(l + wa[nt] + ks * 2048, l + wa[nt] + ks * 2048 + 512);
        acc[nt][0] = mfma(wf, d[2 * ks], acc[nt][0]);
        acc[nt][1] = mfma(wf, d[2 * ks + 1], acc[nt][1]);
      }
    asm volatile("s_waitcnt lgkmcnt(0)" ::: "memory");
  };
  stage(0); loado(0, of[0]); stage(1); loado(1, of[1]);
#pragma unroll
  for (int t = 0; t < 16; ++t) {
    if (t + 2 < 16) { stage(t + 2); loado(t + 2, of[(t + 2) % 3]); WAITV(32); }
    else if (t == 14) WAITV(16); else WAITV(0);
    comp(t, of[t % 3]);
  }
  BARRIER();
#pragma unroll
  for (int nt = 0; nt < 2; ++nt)
#pragma unroll
    for (int qt = 0; qt < 2; ++qt)
#pragma unroll
      for (int g = 0; g < 4; ++g)
        *(LAS f32x4*)(smem + ((w * 4 + nt * 2 + qt) * 4 + g) * 1024 + lane * 16) =
            f32x4{acc[nt][qt][4 * g], acc[nt][qt][4 * g + 1], acc[nt][qt][4 * g + 2], acc[nt][qt][4 * g + 3]};
  BARRIER();
  f32x16 r;
#pragma unroll
  for (int g = 0; g < 4; ++g) {
    f32x4 a = {};
#pragma unroll
    for (int ww = 0; ww < 4; ++ww) a += *(LAS f32x4*)(smem + ((ww * 4 + w) * 4 + g) * 1024 + lane * 16);
    r[4 * g] = a.x; r[4 * g + 1] = a.y; r[4 * g + 2] = a.z; r[4 * g + 3] = a.w;
  }
  const int row = rt * 64 + (w & 1) * 32 + (lane & 31);
  GAS u32x4* dst = (GAS u32x4*)((char*)p.out + (size_t)row * (2 * DM * 2) + (br * DM + n0 + (w >> 1) * 32 + hh * 8) * 2);
  dst[0] = pack8(r, 0);
  dst[2] = pack8(r, 1);
}

template <int NT> __global__ void __launch_bounds__(256, 1) probe_k(Params p) {
  extern __shared__ __attribute__((aligned(16))) char smem_[];
  const unsigned long long t0 = __builtin_amdgcn_s_memrealtime();
  const unsigned sbase = (unsigned)(uintptr_t)(LAS char*)smem_;
  const int bid = blockIdx.x, lane = threadIdx.x & 63, w = __builtin_amdgcn_readfirstlane(threadIdx.x >> 6);
  const int br = bid & 1, c = bid >> 1;
  const void* ptr[5] = {p.x[br], p.k[br], p.v[br], p.wq[br], p.wo[br]};
  const unsigned kb[5] = {8, 32, 32, 64, 64};
#pragma unroll
  for (int a = 0; a < 5; ++a) {
    const u32x4 r = make_rsrc(ptr[a], kb[a] * 1024 * 128);
    for (unsigned i = w; i < kb[a]; i += 4) {
      const unsigned l = sbase + (i & 63) * 1024, so = (c * kb[a] + i) * 1024, vo = lane * 16;
      if (NT) asm volatile("s_mov_b32 m0, %0\n\ts_nop 0\n\tbuffer_load_dwordx4 %1, %2, %3 offen nt lds" :: "s"(l), "v"(vo), "s"(r), "s"(so) : "memory");
      else asm volatile("s_mov_b32 m0, %0\n\ts_nop 0\n\tbuffer_load_dwordx4 %1, %2, %3 offen lds" :: "s"(l), "v"(vo), "s"(r), "s"(so) : "memory");
      WAITV(12);
    }
  }
  WAITV(0);
  BARRIER();
  const unsigned long long t1 = __builtin_amdgcn_s_memrealtime();
  if (bid==100) { const unsigned long long until = t0 + 50 * (t1 - t0);
  while (__builtin_amdgcn_s_memrealtime() < until) __builtin_amdgcn_s_sleep(8); }
}
extern "C" void kernel_launch(void* const* d_in, const int* in_sizes, int n_in, void* d_out, int out_size, void* d_ws,
                              size_t ws_size, hipStream_t stream) {
  Params p;
  p.x[0] = d_in[0]; p.k[0] = d_in[1]; p.v[0] = d_in[2]; p.wq[0] = d_in[3]; p.wo[0] = d_in[4];
  p.x[1] = d_in[5]; p.k[1] = d_in[6]; p.v[1] = d_in[7]; p.wq[1] = d_in[8]; p.wo[1] = d_in[9];
  p.out = d_out; p.ws = d_ws;
  hipFuncSetAttribute((const void*)k1_qattn, hipFuncAttributeMaxDynamicSharedMemorySize, K1_LDS);
  hipFuncSetAttribute((const void*)k2_oproj, hipFuncAttributeMaxDynamicSharedMemorySize, K2_LDS);
  hipFuncSetAttribute((const void*)probe_k<1>, hipFuncAttributeMaxDynamicSharedMemorySize, 131072);
  probe_k<1><<<256, 256, 131072, stream>>>(p);
  k1_qattn<<<256, 256, K1_LDS, stream>>>(p);
  k2_oproj<<<256, 256, K2_LDS, stream>>>(p);
}
```

```cpp
#include <hip/hip_runtime.h>
#include <hip/hip_bf16.h>
#include <stdint.h>

#define DI __device__ __forceinline__
#define LAS __attribute__((address_space(3)))
#define GAS __attribute__((address_space(1)))
typedef float f32x16 __attribute__((ext_vector_type(16)));
typedef float f32x4 __attribute__((ext_vector_type(4)));
typedef float f32x2 __attribute__((ext_vector_type(2)));
typedef short s16x4 __attribute__((ext_vector_type(4)));
typedef short s16x8 __attribute__((ext_vector_type(8)));
typedef __bf16 bf16x8 __attribute__((ext_vector_type(8)));
typedef __bf16 bf16x2 __attribute__((ext_vector_type(2)));
typedef unsigned u32x4 __attribute__((ext_vector_type(4)));
typedef unsigned u32x2 __attribute__((ext_vector_type(2)));

constexpr int NB = 16, SQ = 32, DM = 1024, NH = 32, SK = 32, DH = 128, HD = NH * DH;

DI u32x4 make_rsrc(const void* p, unsigned bytes) {
  uint64_t a = (uint64_t)p;
  u32x4 r;
  r.x = __builtin_amdgcn_readfirstlane((unsigned)a);
  r.y = __builtin_amdgcn_readfirstlane((unsigned)(a >> 32)) & 0xffffu;
  r.z = bytes;
  r.w = 0x00020000u;
  return r;
}
DI void dma16(unsigned lds, unsigned voff, u32x4 rsrc, unsigned soff) {
  asm volatile("s_mov_b32 m0, %0\n\ts_nop 0\n\tbuffer_load_dwordx4 %1, %2, %3 offen lds"
               :: "s"(lds), "v"(voff), "s"(rsrc), "s"(soff) : "memory");
}
#define WAITV(n) asm volatile("s_waitcnt vmcnt(" #n ")" ::: "memory")
#define BARRIER() asm volatile("s_waitcnt lgkmcnt(0)\n\ts_barrier" ::: "memory")
DI f32x16 mfma(u32x4 a, u32x4 b, f32x16 c) {
  return __builtin_amdgcn_mfma_f32_32x32x16_bf16(__builtin_bit_cast(bf16x8, a), __builtin_bit_cast(bf16x8, b), c, 0, 0, 0);
}
DI u32x2 trread(LAS char* p) { return __builtin_bit_cast(u32x2, __builtin_amdgcn_ds_read_tr16_b64_v4i16((LAS s16x4*)p)); }
DI u32x4 trread2(LAS char* p0, LAS char* p1) { u32x2 a = trread(p0), b = trread(p1); return u32x4{a.x, a.y, b.x, b.y}; }
DI unsigned cvtpk(float lo, float hi) { f32x2 v = {lo, hi}; return __builtin_bit_cast(unsigned, __builtin_convertvector(v, bf16x2)); }
DI u32x4 pack8(const f32x16& x, int s) {
  return u32x4{cvtpk(x[8 * s], x[8 * s + 1]), cvtpk(x[8 * s + 2], x[8 * s + 3]), cvtpk(x[8 * s + 4], x[8 * s + 5]), cvtpk(x[8 * s + 6], x[8 * s + 7])};
}

struct Params {
  const void* x[2]; const void* k[2]; const void* v[2]; const void* wq[2]; const void* wo[2];
  void* out; void* ws;
};

constexpr int K1_STAGE = 32768, K1_NS = 4, K1_VOFF = K1_STAGE * K1_NS, K1_LDS = K1_VOFF + 32768;

__global__ void __launch_bounds__(256, 1) k1_qattn(Params p) {
  extern __shared__ __attribute__((aligned(16))) char smem_[];
  LAS char* smem = (LAS char*)smem_;
  const unsigned sbase = (unsigned)(uintptr_t)smem;
  const int bid = blockIdx.x, xcd = bid & 7, i = bid >> 3;
  const int br = xcd >> 2, h = (xcd & 3) * 8 + (i & 7), rg = i >> 3;
  const int tid = threadIdx.x, lane = tid & 63, w = __builtin_amdgcn_readfirstlane(tid >> 6);
  const int b = rg * 4 + w;
  const u32x4 rx = make_rsrc(p.x[br], NB * SQ * DM * 2), rw = make_rsrc(p.wq[br], DM * HD * 2);
  const u32x4 rv = make_rsrc(p.v[br], NB * NH * SK * DH * 2);
  unsigned vx[4], vw[4];
#pragma unroll
  for (int j = 0; j < 4; ++j) {
    int r = 8 * j + (lane >> 3);
    vx[j] = (b * SQ + r) * (DM * 2) + (((lane & 7) ^ ((r >> 1) & 7)) << 4);
    int kr = 16 * w + 4 * j + (lane >> 4);
    vw[j] = kr * (HD * 2) + h * (DH * 2) + (((lane & 15) ^ ((kr & 3) << 2)) << 4);
  }
  auto stage = [&](int t) {
    const unsigned l = sbase + (t & (K1_NS - 1)) * K1_STAGE;
#pragma unroll
    for (int j = 0; j < 4; ++j) dma16(l + w * 4096 + j * 1024, vx[j], rx, t * 128);
#pragma unroll
    for (int j = 0; j < 4; ++j) dma16(l + 16384 + w * 4096 + j * 1024, vw[j], rw, t * 64 * (HD * 2));
  };
  stage(0); stage(1); stage(2);
  const int q = lane & 31, hh = lane >> 5;
  const int tq = (lane & 15) >> 2, tp = lane & 3, tblk = (lane >> 4) & 1;
  const int tps = ((tp & 1) << 1) | (tp >> 1);
  unsigned xa[4];
#pragma unroll
  for (int ks = 0; ks < 4; ++ks) xa[ks] = w * 4096 + q * 128 + (((2 * ks + hh) ^ ((q >> 1) & 7)) << 4);
  unsigned wa[4];
#pragma unroll
  for (int dt = 0; dt < 4; ++dt) wa[dt] = 16384 + (8 * hh + tq) * 256 + ((dt ^ tq) << 6) + tblk * 32 + tps * 8;
  f32x16 acc[4] = {};
  for (int t = 0; t < 16; ++t) {
    if (t < 14) WAITV(16); else if (t == 14) WAITV(16); else WAITV(8);
    BARRIER();
    if (t + 3 < 16) stage(t + 3);
    else if (t == 13) {
#pragma unroll
      for (int j = 0; j < 8; ++j) {
        int key = 4 * j + (lane >> 4);
        dma16(sbase + K1_VOFF + w * 8192 + j * 1024, lane * 0 + (key * 256 + (((lane & 15) ^ ((key & 3) << 2)) << 4)), rv, (b * NH + h) * (SK * DH * 2));
      }
    }
    LAS char* l = smem + (t & (K1_NS - 1)) * K1_STAGE;
#pragma unroll
    for (int ks = 0; ks < 4; ++ks) {
      u32x4 xf = *(LAS u32x4*)(l + xa[ks]);
#pragma unroll
      for (int dt = 0; dt < 4; ++dt) {
        u32x4 wf = trread2(l + wa[dt] + ks * 4096, l + wa[dt] + ks * 4096 + 1024);
        acc[dt] = mfma(wf, xf, acc[dt]);
      }
    }
  }
  const GAS u32x4* kp = (const GAS u32x4*)((const char*)p.k[br] + (size_t)(b * NH + h) * (SK * DH * 2) + q * 256 + hh * 16);
  f32x16 s = {};
#pragma unroll
  for (int dt = 0; dt < 4; ++dt)
#pragma unroll
    for (int ss = 0; ss < 2; ++ss) s = mfma(kp[(dt * 2 + ss) * 2], pack8(acc[dt], ss), s);
  float m = s[0];
#pragma unroll
  for (int r = 1; r < 16; ++r) m = fmaxf(m, s[r]);
  m = fmaxf(m, __shfl_xor(m, 32));
  const float c = 0.08838834764831845f * 1.4426950408889634f;
  float sum = 0.f;
#pragma unroll
  for (int r = 0; r < 16; ++r) { s[r] = __builtin_amdgcn_exp2f((s[r] - m) * c); sum += s[r]; }
  sum += __shfl_xor(sum, 32);
  const float inv = 1.0f / sum;
#pragma unroll
  for (int r = 0; r < 16; ++r) s[r] *= inv;
  const u32x4 p0 = pack8(s, 0), p1 = pack8(s, 1);
  WAITV(0);
  LAS char* vl = smem + K1_VOFF + w * 8192 + (4 * hh + tq) * 256 + tblk * 32 + tps * 8;
  GAS u32x4* op = (GAS u32x4*)((char*)p.ws + ((size_t)(br * NB + b) * 256 + h * 8) * 1024 + lane * 16);
#pragma unroll
  for (int dt = 0; dt < 4; ++dt) {
    LAS char* a = vl + ((dt ^ tq) << 6);
    f32x16 o = {};
    o = mfma(trread2(a, a + 8 * 256), p0, o);
    o = mfma(trread2(a + 16 * 256, a + 24 * 256), p1, o);
    op[(dt * 2) * 64] = pack8(o, 0);
    op[(dt * 2 + 1) * 64] = pack8(o, 1);
  }
}

constexpr int K2_STAGE = 8192, K2_NS = 4, K2_LDS = 4 * K2_NS * K2_STAGE;
__global__ void __launch_bounds__(256, 1) k2_oproj(Params p) {
  extern __shared__ __attribute__((aligned(16))) char smem_[];
  LAS char* smem = (LAS char*)smem_;
  const unsigned sbase = (unsigned)(uintptr_t)smem;
  const int bid = blockIdx.x, xcd = bid & 7, i = bid >> 3;
  const int br = xcd >> 2, n0 = (xcd & 3) * 256 + (i & 3) * 64, rt = i >> 2;
  const int tid = threadIdx.x, lane = tid & 63, w = __builtin_amdgcn_readfirstlane(tid >> 6);
  const u32x4 rw = make_rsrc(p.wo[br], HD * DM * 2);
  const int kr = lane >> 3;
  const unsigned vw = (w * 1024 + kr) * (DM * 2) + n0 * 2 + (((lane & 7) ^ (((kr >> 1) & 1) << 2)) << 4);
  auto stage = [&](int t) {
    const unsigned l = sbase + (w * K2_NS + (t & (K2_NS - 1))) * K2_STAGE;
#pragma unroll
    for (int j = 0; j < 8; ++j) dma16(l + j * 1024, vw, rw, (t * 64 + j * 8) * (DM * 2));
  };
  const GAS u32x4* op = (const GAS u32x4*)((const char*)p.ws + ((size_t)(br * NB + rt * 2) * 256 + w * 64) * 1024 + lane * 16);
  const int hh = lane >> 5, tq = (lane & 15) >> 2, tp = lane & 3, tblk = (lane >> 4) & 1;
  const int tps = ((tp & 1) << 1) | (tp >> 1);
  unsigned wa[2];
#pragma unroll
  for (int nt = 0; nt < 2; ++nt) wa[nt] = (8 * hh + tq) * 128 + ((nt ^ (tq >> 1)) << 6) + tblk * 32 + tps * 8;
  f32x16 acc[2][2] = {};
  u32x4 of[3][8];
  auto loado = [&](int t, u32x4 (&d)[8]) {
#pragma unroll
    for (int ks = 0; ks < 4; ++ks) {
      asm volatile("global_load_dwordx4 %0, %1, off" : "=v"(d[2 * ks]) : "v"(op + (t * 4 + ks) * 64) : "memory");
      asm volatile("global_load_dwordx4 %0, %1, off" : "=v"(d[2 * ks + 1]) : "v"(op + (256 + t * 4 + ks) * 64) : "memory");
    }
  };
  auto comp = [&](int t, u32x4 (&d)[8]) {
    asm volatile("" : "+v"(d[0]), "+v"(d[1]), "+v"(d[2]), "+v"(d[3]), "+v"(d[4]), "+v"(d[5]), "+v"(d[6]), "+v"(d[7]));
    LAS char* l = smem + (w * K2_NS + (t & (K2_NS - 1))) * K2_STAGE;
#pragma unroll
    for (int ks = 0; ks < 4; ++ks)
#pragma unroll
      for (int nt = 0; nt < 2; ++nt) {
        u32x4 wf = trread2(l + wa[nt] + ks * 2048, l + wa[nt] + ks * 2048 + 512);
        acc[nt][0] = mfma(wf, d[2 * ks], acc[nt][0]);
        acc[nt][1] = mfma(wf, d[2 * ks + 1], acc[nt][1]);
      }
    asm volatile("s_waitcnt lgkmcnt(0)" ::: "memory");
  };
  stage(0); loado(0, of[0]); stage(1); loado(1, of[1]);
#pragma unroll
  for (int t = 0; t < 16; ++t) {
    if (t + 2 < 16) { stage(t + 2); loado(t + 2, of[(t + 2) % 3]); WAITV(32); }
    else if (t == 14) WAITV(16); else WAITV(0);
    comp(t, of[t % 3]);
  }
  BARRIER();
#pragma unroll
  for (int nt = 0; nt < 2; ++nt)
#pragma unroll
    for (int qt = 0; qt < 2; ++qt)
#pragma unroll
      for (int g = 0; g < 4; ++g)
        *(LAS f32x4*)(smem + ((w * 4 + nt * 2 + qt) * 4 + g) * 1024 + lane * 16) =
            f32x4{acc[nt][qt][4 * g], acc[nt][qt][4 * g + 1], acc[nt][qt][4 * g + 2], acc[nt][qt][4 * g + 3]};
  BARRIER();
  f32x16 r;
#pragma unroll
  for (int g = 0; g < 4; ++g) {
    f32x4 a = {};
#pragma unroll
    for (int ww = 0; ww < 4; ++ww) a += *(LAS f32x4*)(smem + ((ww * 4 + w) * 4 + g) * 1024 + lane * 16);
    r[4 * g] = a.x; r[4 * g + 1] = a.y; r[4 * g + 2] = a.z; r[4 * g + 3] = a.w;
  }
  const int row = rt * 64 + (w & 1) * 32 + (lane & 31);
  GAS u32x4* dst = (GAS u32x4*)((char*)p.out + (size_t)row * (2 * DM * 2) + (br * DM + n0 + (w >> 1) * 32 + hh * 8) * 2);
  dst[0] = pack8(r, 0);
  dst[2] = pack8(r, 1);
}

__global__ void __launch_bounds__(256, 1) probe_k(Params p) {
  extern __shared__ __attribute__((aligned(16))) char smem_[];
  const unsigned long long t0 = __builtin_amdgcn_s_memrealtime();
  const unsigned sbase = (unsigned)(uintptr_t)(LAS char*)smem_;
  const int bid = blockIdx.x, lane = threadIdx.x & 63, w = __builtin_amdgcn_readfirstlane(threadIdx.x >> 6);
  unsigned long long wait_ticks = 0;


  { const unsigned long long until = __builtin_amdgcn_s_memrealtime() + (wait_ticks > 200000 ? 200000 : wait_ticks);
  while (__builtin_amdgcn_s_memrealtime() < until) __builtin_amdgcn_s_sleep(8); }
}
extern "C" void kernel_launch(void* const* d_in, const int* in_sizes, int n_in, void* d_out, int out_size, void* d_ws,
                              size_t ws_size, hipStream_t stream) {
  Params p;
  p.x[0] = d_in[0]; p.k[0] = d_in[1]; p.v[0] = d_in[2]; p.wq[0] = d_in[3]; p.wo[0] = d_in[4];
  p.x[1] = d_in[5]; p.k[1] = d_in[6]; p.v[1] = d_in[7]; p.wq[1] = d_in[8]; p.wo[1] = d_in[9];
  p.out = d_out; p.ws = d_ws;
  hipFuncSetAttribute((const void*)k1_qattn, hipFuncAttributeMaxDynamicSharedMemorySize, K1_LDS);
  hipFuncSetAttribute((const void*)k2_oproj, hipFuncAttributeMaxDynamicSharedMemorySize, K2_LDS);
  hipFuncSetAttribute((const void*)probe_k, hipFuncAttributeMaxDynamicSharedMemorySize, 131072);
  probe_k<<<256, 256, 131072, stream>>>(p);
  k1_qattn<<<256, 256, K1_LDS, stream>>>(p);
  k2_oproj<<<256, 256, K2_LDS, stream>>>(p);
}
```

```cpp
#include <hip/hip_runtime.h>
#include <stdint.h>

struct Branch { const void *x, *wq, *k, *v, *wo; void *out, *ws, *ws2; };
struct Params { Branch b[2]; };

__global__ void __launch_bounds__(512, 2) mha2_fused(Params) {
  unsigned tid = threadIdx.x;
  asm volatile(
    "L_start:\n"
    "s_bfe_u32 s5, %1, 0x10002\n"
    "s_lshl_b32 s12, s5, 6\n"
    "s_load_dwordx16 s[16:31], %0, s12 offset:0x0\n"
    "v_lshrrev_b32 v2, 6, v0\n"
    "v_and_b32 v1, 63, v0\n"
    "v_readfirstlane_b32 s14, v2\n"
    "s_and_b32 s6, %1, 3\n"
    "s_bfe_u32 s7, %1, 0x30003\n"
    "s_lshr_b32 s8, %1, 6\n"
    "s_lshr_b32 s9, s7, 1\n"
    "s_sub_u32 s9, s8, s9\n"
    "s_and_b32 s9, s9, 3\n"
    "s_and_b32 s4, s14, 3\n"
    "s_lshl2_add_u32 s11, s8, s4\n"
    "s_min_u32 s33, s9, 3\n"
    "s_add_u32 s33, s33, 12\n"
    "s_mov_b32 s15, 0x100000\n"
    "s_lshl4_add_u32 s34, s5, s11\n"
    "s_lshl2_add_u32 s35, s34, s6\n"
    "s_lshl3_add_u32 s35, s35, s7\n"
    "s_lshl_b32 s35, s35, 13\n"
    "s_add_u32 s35, s35, 0x100000\n"
    "s_cmp_lt_u32 s14, 4\n"
    "s_cbranch_scc1 L_compute\n"
    "s_setprio 2\n"
    "v_lshrrev_b32 v41, 4, v1\n"
    "v_and_b32 v43, 15, v1\n"
    "v_lshrrev_b32 v48, 5, v1\n"
    "v_and_b32 v46, 1, v41\n"
    "v_lshlrev_b32 v45, 4, v48\n"
    "v_lshlrev_b32 v47, 4, v43\n"
    "v_lshl_add_u32 v45, v46, 2, v45\n"
    "v_lshrrev_b32 v40, 3, v1\n"
    "v_and_b32 v42, 7, v1\n"
    "v_xor_b32 v42, v42, v41\n"
    "v_lshlrev_b32 v42, 4, v42\n"
    "v_xor_b32 v44, 64, v42\n"
    "v_lshl_add_u32 v8, v45, 13, v47\n"
    "v_add_u32 v9, 0x1be0, v8\n"
    "v_add_u32 v10, 0x37c0, v8\n"
    "v_add_u32 v11, 0x53a0, v8\n"
    "v_lshl_add_u32 v12, v40, 11, v42\n"
    "v_lshl_add_u32 v13, v40, 11, v44\n"
    "v_add_u32 v14, 0x7800, v12\n"
    "v_add_u32 v15, 0xb400, v13\n"
    "v_add_u32 v13, 0x3c00, v13\n"
    "s_mov_b32 s52, 0x54710b23\n"
    "s_mov_b32 s53, 0xfeda98c6\n"
    "s_cmp_eq_u32 s9, 1\n"
    "s_cselect_b32 s52, 0x5821043b, s52\n"
    "s_cselect_b32 s53, 0xfeca9d76, s53\n"
    "s_cmp_eq_u32 s9, 2\n"
    "s_cselect_b32 s52, 0x94215b30, s52\n"
    "s_cselect_b32 s53, 0xfdcae876, s53\n"
    "s_cmp_eq_u32 s9, 3\n"
    "s_cselect_b32 s52, 0x54260b31, s52\n"
    "s_cselect_b32 s53, 0xedcf987a, s53\n"
    "s_lshr_b32 s12, s4, 1\n"
    "s_and_b32 s13, s4, 1\n"
    "s_lshl2_add_u32 s14, s12, s13\n"
    "s_lshl3_add_u32 s88, s6, s7\n"
    "s_lshl_b32 s12, s88, 8\n"
    "s_lshl_b32 s63, s14, 16\n"
    "s_add_u32 s63, s63, s12\n"
    "s_lshl_b32 s64, s11, 16\n"
    "s_lshl_b32 s56, s4, 12\n"
    "s_mul_i32 s57, s4, 4224\n"
    "s_add_u32 s57, s57, 0x4000\n"
    "s_mov_b32 s54, 0\n"
    "s_mov_b32 s55, 0\n"
    "s_mov_b32 s65, s35\n"
    "s_mov_b32 s62, 2\n"
    "v_lshlrev_b32 v20, 4, v1\n"
    "v_mov_b64 v[24:25], -1\n"
    "v_mov_b64 v[26:27], -1\n"
    "s_mov_b32 s42, 0x800000\n"
    "s_mov_b32 s43, 0x20000\n"
    "s_mov_b32 s46, 0x100000\n"
    "s_mov_b32 s47, 0x20000\n"
    "s_mov_b32 s38, 0x1000000\n"
    "s_mov_b32 s39, 0x20000\n"
    "s_waitcnt lgkmcnt(0)\n"
    "s_mov_b32 s40, s18\n"
    "s_and_b32 s41, s19, 0xffff\n"
    "s_mov_b32 s44, s16\n"
    "s_and_b32 s45, s17, 0xffff\n"
    "s_mov_b32 s36, s28\n"
    "s_and_b32 s37, s29, 0xffff\n"
    "L_la:\n"
    "s_and_b32 s59, s52, 15\n"
    "s_lshr_b64 s[52:53], s[52:53], 4\n"
    "s_lshl_b32 s60, s59, 19\n"
    "s_lshl_b32 s61, s59, 7\n"
    "s_add_u32 s60, s60, s63\n"
    "s_add_u32 s61, s61, s64\n"
    "s_add_u32 m0, s55, s57\n"
    "s_nop 0\n"
    "buffer_load_dwordx4 v8, s[40:43], s60 offen offset:0 lds\n"
    "buffer_load_dwordx4 v9, s[40:43], s60 offen offset:1056 lds\n"
    "buffer_load_dwordx4 v10, s[40:43], s60 offen offset:2112 lds\n"
    "buffer_load_dwordx4 v11, s[40:43], s60 offen offset:3168 lds\n"
    "s_cmp_lt_u32 s54, 2\n"
    "s_cbranch_scc1 L_la_nb\n"
    "s_waitcnt vmcnt(13)\n"
    "s_barrier\n"
    "L_la_nb:\n"
    "buffer_store_dwordx4 v[24:27], v20, s[36:39], s65 offen sc1\n"
    "s_add_u32 s65, s65, 0x400\n"
    "s_add_u32 m0, s55, s56\n"
    "s_nop 0\n"
    "buffer_load_dwordx4 v12, s[44:47], s61 offen offset:0 lds\n"
    "buffer_load_dwordx4 v13, s[44:47], s61 offen offset:1024 lds\n"
    "buffer_load_dwordx4 v14, s[44:47], s61 offen offset:2048 lds\n"
    "buffer_load_dwordx4 v15, s[44:47], s61 offen offset:3072 lds\n"
    "s_add_u32 s55, s55, 0x8200\n"
    "s_cmp_eq_u32 s55, 0x20800\n"
    "s_cselect_b32 s55, 0, s55\n"
    "s_add_u32 s54, s54, 1\n"
    "s_cmp_lt_u32 s54, s62\n"
    "s_cbranch_scc1 L_la\n"
    "s_cmp_eq_u32 s54, 2\n"
    "s_cbranch_scc1 L_lsetup\n"
    "s_mov_b32 s62, 10\n"
    "L_lb:\n"
    "s_and_b32 s59, s52, 15\n"
    "s_lshr_b64 s[52:53], s[52:53], 4\n"
    "s_lshl_b32 s60, s59, 19\n"
    "s_lshl_b32 s61, s59, 7\n"
    "s_add_u32 s60, s60, s63\n"
    "s_add_u32 s61, s61, s64\n"
    "s_add_u32 m0, s55, s57\n"
    "s_nop 0\n"
    "buffer_load_dwordx4 v8, s[40:43], s60 offen offset:0 lds\n"
    "buffer_load_dwordx4 v9, s[40:43], s60 offen offset:1056 lds\n"
    "buffer_load_dwordx4 v10, s[40:43], s60 offen offset:2112 lds\n"
    "buffer_load_dwordx4 v11, s[40:43], s60 offen offset:3168 lds\n"
    "s_waitcnt vmcnt(12)\n"
    "s_barrier\n"
    "s_add_u32 m0, s55, s56\n"
    "s_nop 0\n"
    "buffer_load_dwordx4 v12, s[44:47], s61 offen offset:0 lds\n"
    "buffer_load_dwordx4 v13, s[44:47], s61 offen offset:1024 lds\n"
    "buffer_load_dwordx4 v14, s[44:47], s61 offen offset:2048 lds\n"
    "buffer_load_dwordx4 v15, s[44:47], s61 offen offset:3072 lds\n"
    "s_add_u32 s55, s55, 0x8200\n"
    "s_cmp_eq_u32 s55, 0x20800\n"
    "s_cselect_b32 s55, 0, s55\n"
    "s_add_u32 s54, s54, 1\n"
    "s_cmp_lt_u32 s54, s62\n"
    "s_cbranch_scc1 L_lb\n"
    "s_cmp_eq_u32 s54, 17\n"
    "s_cbranch_scc1 L_l2pre\n"
    "s_cmp_eq_u32 s54, 10\n"
    "s_cbranch_scc0 L_lk\n"
    "v_readfirstlane_b32 s68, v28\n"
    "v_readfirstlane_b32 s69, v29\n"
    "s_add_u32 s68, s68, 1\n"
    "s_addc_u32 s69, s69, 0\n"
    "v_mov_b32 v24, s68\n"
    "v_mov_b32 v25, s69\n"
    "s_mov_b64 exec, 1\n"
    "buffer_store_dwordx2 v[24:25], off, s[36:39], s88 sc1\n"
    "s_mov_b64 exec, -1\n"
    "s_mov_b32 s62, s33\n"
    "s_branch L_lb\n"
    "L_lk:\n"
    "s_add_u32 m0, s55, s67\n"
    "s_nop 0\n"
    "buffer_load_dwordx4 v16, s[48:51], s87 offen offset:0 nt lds\n"
    "buffer_load_dwordx4 v17, s[48:51], s87 offen offset:1024 nt lds\n"
    "buffer_load_dwordx4 v18, s[48:51], s87 offen offset:2048 nt lds\n"
    "buffer_load_dwordx4 v19, s[48:51], s87 offen offset:3072 nt lds\n"
    "s_waitcnt vmcnt(12)\n"
    "s_barrier\n"
    "s_add_u32 m0, s55, s56\n"
    "s_nop 0\n"
    "buffer_load_dwordx4 v16, s[48:51], s86 offen offset:0 nt lds\n"
    "buffer_load_dwordx4 v17, s[48:51], s86 offen offset:1024 nt lds\n"
    "buffer_load_dwordx4 v18, s[48:51], s86 offen offset:2048 nt lds\n"
    "buffer_load_dwordx4 v19, s[48:51], s86 offen offset:3072 nt lds\n"
    "s_add_u32 s55, s55, 0x8200\n"
    "s_cmp_eq_u32 s55, 0x20800\n"
    "s_cselect_b32 s55, 0, s55\n"
    "s_add_u32 s54, s54, 1\n"
    "s_mov_b32 s62, 17\n"
    "s_branch L_lb\n"
    "L_l2pre:\n"
    "buffer_load_dwordx2 v[28:29], v30, s[36:39], s66 offen sc1\n"
    "s_mov_b32 s62, 19\n"
    "L_l2:\n"
    "s_and_b32 s60, s70, 15\n"
    "s_lshl_b32 s60, s60, 17\n"
    "s_add_u32 s60, s60, s84\n"
    "s_add_u32 m0, s55, s57\n"
    "s_nop 0\n"
    "buffer_load_dwordx4 v32, s[72:75], s60 offen offset:0 lds\n"
    "buffer_load_dwordx4 v33, s[72:75], s60 offen offset:1056 lds\n"
    "buffer_load_dwordx4 v34, s[72:75], s60 offen offset:2112 lds\n"
    "buffer_load_dwordx4 v35, s[72:75], s60 offen offset:3168 lds\n"
    "s_bfe_u32 s58, s70, 0x30001\n"
    "s_and_b32 s59, s70, 1\n"
    "s_lshr_b64 s[70:71], s[70:71], 4\n"
    "s_lshl_b32 s58, s58, 13\n"
    "s_lshl_b32 s59, s59, 7\n"
    "s_add_u32 s61, s58, s85\n"
    "s_add_u32 s61, s61, s59\n"
    "s_waitcnt vmcnt(12)\n"
    "s_barrier\n"
    "s_add_u32 m0, s55, s56\n"
    "s_nop 0\n"
    "buffer_load_dwordx4 v16, s[76:79], s61 offen offset:0 lds\n"
    "buffer_load_dwordx4 v17, s[76:79], s61 offen offset:1024 lds\n"
    "buffer_load_dwordx4 v18, s[76:79], s61 offen offset:2048 lds\n"
    "buffer_load_dwordx4 v19, s[76:79], s61 offen offset:3072 lds\n"
    "s_add_u32 s55, s55, 0x8200\n"
    "s_cmp_eq_u32 s55, 0x20800\n"
    "s_cselect_b32 s55, 0, s55\n"
    "s_add_u32 s54, s54, 1\n"
    "s_cmp_lt_u32 s54, s62\n"
    "s_cbranch_scc1 L_l2\n"
    "s_cmp_eq_u32 s54, 19\n"
    "s_cbranch_scc1 L_hs\n"
    "s_mov_b32 s62, 32\n"
    "L_l3:\n"
    "s_and_b32 s60, s70, 15\n"
    "s_lshl_b32 s60, s60, 17\n"
    "s_add_u32 s60, s60, s84\n"
    "s_add_u32 m0, s55, s57\n"
    "s_nop 0\n"
    "buffer_load_dwordx4 v32, s[72:75], s60 offen offset:0 lds\n"
    "buffer_load_dwordx4 v33, s[72:75], s60 offen offset:1056 lds\n"
    "buffer_load_dwordx4 v34, s[72:75], s60 offen offset:2112 lds\n"
    "buffer_load_dwordx4 v35, s[72:75], s60 offen offset:3168 lds\n"
    "s_bfe_u32 s58, s70, 0x30001\n"
    "s_and_b32 s59, s70, 1\n"
    "s_lshr_b64 s[70:71], s[70:71], 4\n"
    "s_lshl_b32 s58, s58, 13\n"
    "s_lshl_b32 s59, s59, 7\n"
    "s_add_u32 s61, s58, s85\n"
    "s_add_u32 s61, s61, s59\n"
    "s_add_u32 m0, s55, s56\n"
    "s_nop 0\n"
    "buffer_load_dwordx4 v16, s[76:79], s61 offen offset:0 lds\n"
    "buffer_load_dwordx4 v17, s[76:79], s61 offen offset:1024 lds\n"
    "buffer_load_dwordx4 v18, s[76:79], s61 offen offset:2048 lds\n"
    "buffer_load_dwordx4 v19, s[76:79], s61 offen offset:3072 lds\n"
    "s_waitcnt vmcnt(16)\n"
    "s_barrier\n"
    "s_add_u32 s55, s55, 0x8200\n"
    "s_cmp_eq_u32 s55, 0x20800\n"
    "s_cselect_b32 s55, 0, s55\n"
    "s_add_u32 s54, s54, 1\n"
    "s_cmp_lt_u32 s54, s62\n"
    "s_cbranch_scc1 L_l3\n"
    "L_l4:\n"
    "s_and_b32 s60, s70, 15\n"
    "s_lshl_b32 s60, s60, 17\n"
    "s_add_u32 s60, s60, s84\n"
    "s_add_u32 m0, s55, s57\n"
    "s_nop 0\n"
    "buffer_load_dwordx4 v32, s[72:75], s60 offen offset:0 lds\n"
    "buffer_load_dwordx4 v33, s[72:75], s60 offen offset:1056 lds\n"
    "buffer_load_dwordx4 v34, s[72:75], s60 offen offset:2112 lds\n"
    "buffer_load_dwordx4 v35, s[72:75], s60 offen offset:3168 lds\n"
    "s_bfe_u32 s58, s70, 0x30001\n"
    "s_and_b32 s59, s70, 1\n"
    "s_lshr_b64 s[70:71], s[70:71], 4\n"
    "s_lshl_b32 s58, s58, 13\n"
    "s_lshl_b32 s59, s59, 7\n"
    "s_add_u32 s61, s58, s85\n"
    "s_add_u32 s61, s61, s59\n"
    "s_waitcnt vmcnt(12)\n"
    "s_barrier\n"
    "s_add_u32 m0, s55, s56\n"
    "s_nop 0\n"
    "buffer_load_dwordx4 v16, s[76:79], s61 offen offset:0 lds\n"
    "buffer_load_dwordx4 v17, s[76:79], s61 offen offset:1024 lds\n"
    "buffer_load_dwordx4 v18, s[76:79], s61 offen offset:2048 lds\n"
    "buffer_load_dwordx4 v19, s[76:79], s61 offen offset:3072 lds\n"
    "s_add_u32 s55, s55, 0x8200\n"
    "s_cmp_eq_u32 s55, 0x20800\n"
    "s_cselect_b32 s55, 0, s55\n"
    "s_add_u32 s54, s54, 1\n"
    "s_waitcnt vmcnt(8)\n"
    "s_barrier\n"
    "s_waitcnt vmcnt(0) lgkmcnt(0)\n"
    "s_barrier\n"
    "L_wet:\n"
    "s_barrier\n"
    "s_sleep 6\n"
    "L_retry:\n"
    "s_sub_u32 s15, s15, 1\n"
    "s_cbranch_scc1 L_exit\n"
    "buffer_load_dwordx4 v[64:67], v20, s[36:39], s89 offen offset:0 sc1\n"
    "buffer_load_dwordx4 v[68:71], v20, s[36:39], s89 offen offset:1024 sc1\n"
    "buffer_load_dwordx4 v[72:75], v20, s[36:39], s90 offen offset:0 sc1\n"
    "buffer_load_dwordx4 v[76:79], v20, s[36:39], s90 offen offset:1024 sc1\n"
    "L_last:\n"
    "buffer_load_dwordx4 v[80:83], v20, s[36:39], s91 offen offset:0 sc1\n"
    "buffer_load_dwordx4 v[84:87], v20, s[36:39], s91 offen offset:1024 sc1\n"
    "ds_read_b128 v[96:99], v38 offset:33280\n"
    "ds_read_b128 v[100:103], v38 offset:34304\n"
    "ds_read_b128 v[104:107], v38 offset:35328\n"
    "ds_read_b128 v[108:111], v38 offset:36352\n"
    "s_waitcnt lgkmcnt(0)\n"
    "s_waitcnt vmcnt(4)\n"
    "v_max3_u32 v136, v64, v65, v66\n"
    "v_max3_u32 v136, v136, v67, v68\n"
    "v_max3_u32 v136, v136, v69, v70\n"
    "v_max_u32 v136, v136, v71\n"
    "v_cmp_eq_u32 vcc, -1, v136\n"
    "s_cbranch_vccnz L_retry\n"
    "v_lshlrev_b32 v128, 16, v64\n"
    "v_and_b32 v129, 0xffff0000, v64\n"
    "v_pk_add_f32 v[96:97], v[96:97], v[128:129]\n"
    "v_lshlrev_b32 v130, 16, v65\n"
    "v_and_b32 v131, 0xffff0000, v65\n"
    "v_pk_add_f32 v[98:99], v[98:99], v[130:131]\n"
    "v_lshlrev_b32 v132, 16, v68\n"
    "v_and_b32 v133, 0xffff0000, v68\n"
    "v_pk_add_f32 v[100:101], v[100:101], v[132:133]\n"
    "v_lshlrev_b32 v134, 16, v69\n"
    "v_and_b32 v135, 0xffff0000, v69\n"
    "v_pk_add_f32 v[102:103], v[102:103], v[134:135]\n"
    "v_lshlrev_b32 v128, 16, v66\n"
    "v_and_b32 v129, 0xffff0000, v66\n"
    "v_pk_add_f32 v[104:105], v[104:105], v[128:129]\n"
    "v_lshlrev_b32 v130, 16, v67\n"
    "v_and_b32 v131, 0xffff0000, v67\n"
    "v_pk_add_f32 v[106:107], v[106:107], v[130:131]\n"
    "v_lshlrev_b32 v132, 16, v70\n"
    "v_and_b32 v133, 0xffff0000, v70\n"
    "v_pk_add_f32 v[108:109], v[108:109], v[132:133]\n"
    "v_lshlrev_b32 v134, 16, v71\n"
    "v_and_b32 v135, 0xffff0000, v71\n"
    "v_pk_add_f32 v[110:111], v[110:111], v[134:135]\n"
    "s_waitcnt vmcnt(2)\n"
    "v_max3_u32 v136, v72, v73, v74\n"
    "v_max3_u32 v136, v136, v75, v76\n"
    "v_max3_u32 v136, v136, v77, v78\n"
    "v_max_u32 v136, v136, v79\n"
    "v_cmp_eq_u32 vcc, -1, v136\n"
    "s_cbranch_vccnz L_retry\n"
    "v_lshlrev_b32 v128, 16, v72\n"
    "v_and_b32 v129, 0xffff0000, v72\n"
    "v_pk_add_f32 v[96:97], v[96:97], v[128:129]\n"
    "v_lshlrev_b32 v130, 16, v73\n"
    "v_and_b32 v131, 0xffff0000, v73\n"
    "v_pk_add_f32 v[98:99], v[98:99], v[130:131]\n"
    "v_lshlrev_b32 v132, 16, v76\n"
    "v_and_b32 v133, 0xffff0000, v76\n"
    "v_pk_add_f32 v[100:101], v[100:101], v[132:133]\n"
    "v_lshlrev_b32 v134, 16, v77\n"
    "v_and_b32 v135, 0xffff0000, v77\n"
    "v_pk_add_f32 v[102:103], v[102:103], v[134:135]\n"
    "v_lshlrev_b32 v128, 16, v74\n"
    "v_and_b32 v129, 0xffff0000, v74\n"
    "v_pk_add_f32 v[104:105], v[104:105], v[128:129]\n"
    "v_lshlrev_b32 v130, 16, v75\n"
    "v_and_b32 v131, 0xffff0000, v75\n"
    "v_pk_add_f32 v[106:107], v[106:107], v[130:131]\n"
    "v_lshlrev_b32 v132, 16, v78\n"
    "v_and_b32 v133, 0xffff0000, v78\n"
    "v_pk_add_f32 v[108:109], v[108:109], v[132:133]\n"
    "v_lshlrev_b32 v134, 16, v79\n"
    "v_and_b32 v135, 0xffff0000, v79\n"
    "v_pk_add_f32 v[110:111], v[110:111], v[134:135]\n"
    "s_waitcnt vmcnt(0)\n"
    "v_max3_u32 v136, v80, v81, v82\n"
    "v_max3_u32 v136, v136, v83, v84\n"
    "v_max3_u32 v136, v136, v85, v86\n"
    "v_max_u32 v136, v136, v87\n"
    "v_cmp_eq_u32 vcc, -1, v136\n"
    "s_cbranch_vccnz L_retry\n"
    "v_lshlrev_b32 v128, 16, v80\n"
    "v_and_b32 v129, 0xffff0000, v80\n"
    "v_pk_add_f32 v[96:97], v[96:97], v[128:129]\n"
    "v_lshlrev_b32 v130, 16, v81\n"
    "v_and_b32 v131, 0xffff0000, v81\n"
    "v_pk_add_f32 v[98:99], v[98:99], v[130:131]\n"
    "v_lshlrev_b32 v132, 16, v84\n"
    "v_and_b32 v133, 0xffff0000, v84\n"
    "v_pk_add_f32 v[100:101], v[100:101], v[132:133]\n"
    "v_lshlrev_b32 v134, 16, v85\n"
    "v_and_b32 v135, 0xffff0000, v85\n"
    "v_pk_add_f32 v[102:103], v[102:103], v[134:135]\n"
    "v_cvt_pk_bf16_f32 v112, v96, v97\n"
    "v_cvt_pk_bf16_f32 v113, v98, v99\n"
    "v_cvt_pk_bf16_f32 v114, v100, v101\n"
    "v_cvt_pk_bf16_f32 v115, v102, v103\n"
    "s_nop 0\n"
    "v_permlane16_swap_b32 v112, v114\n"
    "v_permlane16_swap_b32 v113, v115\n"
    "buffer_store_dwordx4 v[112:115], v36, s[80:83], s92 offen sc1\n"
    "v_lshlrev_b32 v128, 16, v82\n"
    "v_and_b32 v129, 0xffff0000, v82\n"
    "v_pk_add_f32 v[104:105], v[104:105], v[128:129]\n"
    "v_lshlrev_b32 v130, 16, v83\n"
    "v_and_b32 v131, 0xffff0000, v83\n"
    "v_pk_add_f32 v[106:107], v[106:107], v[130:131]\n"
    "v_lshlrev_b32 v132, 16, v86\n"
    "v_and_b32 v133, 0xffff0000, v86\n"
    "v_pk_add_f32 v[108:109], v[108:109], v[132:133]\n"
    "v_lshlrev_b32 v134, 16, v87\n"
    "v_and_b32 v135, 0xffff0000, v87\n"
    "v_pk_add_f32 v[110:111], v[110:111], v[134:135]\n"
    "v_cvt_pk_bf16_f32 v116, v104, v105\n"
    "v_cvt_pk_bf16_f32 v117, v106, v107\n"
    "v_cvt_pk_bf16_f32 v118, v108, v109\n"
    "v_cvt_pk_bf16_f32 v119, v110, v111\n"
    "s_nop 0\n"
    "v_permlane16_swap_b32 v116, v118\n"
    "v_permlane16_swap_b32 v117, v119\n"
    "buffer_store_dwordx4 v[116:119], v36, s[80:83], s93 offen sc1\n"
    "s_endpgm\n"
    "L_hs_spin:\n"
    "s_sub_u32 s15, s15, 1\n"
    "s_cbranch_scc1 L_exit\n"
    "buffer_load_dwordx2 v[28:29], v30, s[36:39], s66 offen sc1\n"
    "s_waitcnt vmcnt(0)\n"
    "L_hs:\n"
    "s_getpc_b64 s[48:49]\n"
    "L_pc2:\n"
    "s_add_u32 s48, s48, L_fin-L_pc2\n"
    "s_addc_u32 s49, s49, 0\n"
    "s_lshr_b32 s12, %1, 3\n"
    "s_lshl2_add_u32 s12, s12, s4\n"
    "s_lshl_b32 s12, s12, 6\n"
    "s_min_u32 s12, s12, L_exit-L_fin\n"
    "s_load_dword s50, s[48:49], s12 offset:0x0\n"
    "s_waitcnt vmcnt(16)\n"
    "s_mov_b32 s62, 31\n"
    "v_cmp_ne_u64 vcc, s[68:69], v[28:29]\n"
    "s_cbranch_vccnz L_hs_spin\n"
    "s_or_b32 s12, s6, s7\n"
    "s_cbranch_scc1 L_l2\n"
    "s_sub_u32 s12, s66, 0x4000\n"
    "s_mov_b64 exec, 1\n"
    "buffer_store_dwordx2 v[24:25], off, s[36:39], s12 sc1\n"
    "s_mov_b64 exec, -1\n"
    "s_branch L_l2\n"
    "L_lsetup:\n"
    "s_lshl_b32 s66, s34, 8\n"
    "buffer_load_dwordx2 v[28:29], off, s[36:39], s66 offset:0 sc1\n"
    "s_add_u32 s66, s66, 0x4000\n"
    "s_lshl3_add_u32 s88, s88, s66\n"
    "s_or_b32 s10, s8, 1\n"
    "s_sub_u32 s10, s7, s10\n"
    "s_and_b32 s10, s10, 7\n"
    "s_add_u32 s12, s6, 1\n"
    "s_lshl_b32 s12, s12, 6\n"
    "v_add_u32 v47, s12, v47\n"
    "v_and_b32 v47, 0xff, v47\n"
    "v_lshl_add_u32 v16, v40, 8, v42\n"
    "v_lshl_add_u32 v17, v40, 8, v44\n"
    "v_add_u32 v18, 0x800, v16\n"
    "v_add_u32 v19, 0xc00, v17\n"
    "v_add_u32 v17, 0x400, v17\n"
    "v_lshl_add_u32 v32, v45, 11, v47\n"
    "v_add_u32 v33, 0x3e0, v32\n"
    "v_add_u32 v34, 0x7c0, v32\n"
    "v_add_u32 v35, 0xba0, v32\n"
    "s_mov_b32 s48, s20\n"
    "s_and_b32 s49, s21, 0xffff\n"
    "s_mov_b32 s50, 0x400000\n"
    "s_mov_b32 s51, 0x20000\n"
    "s_mov_b32 s72, s24\n"
    "s_and_b32 s73, s25, 0xffff\n"
    "s_mov_b32 s74, 0x800000\n"
    "s_mov_b32 s75, 0x20000\n"
    "s_mov_b32 s76, s22\n"
    "s_and_b32 s77, s23, 0xffff\n"
    "s_mov_b32 s78, 0x400000\n"
    "s_mov_b32 s79, 0x20000\n"
    "s_mov_b32 s80, s26\n"
    "s_and_b32 s81, s27, 0xffff\n"
    "s_mov_b32 s82, 0x200000\n"
    "s_mov_b32 s83, 0x20000\n"
    "s_mov_b32 s70, 0x98543210\n"
    "s_mov_b32 s71, 0xfebadc76\n"
    "s_cmp_eq_u32 s8, 1\n"
    "s_cselect_b32 s70, 0x89452301, s70\n"
    "s_cselect_b32 s71, 0xefabcd67, s71\n"
    "s_cmp_eq_u32 s8, 2\n"
    "s_cselect_b32 s70, 0x32761054, s70\n"
    "s_cselect_b32 s71, 0xdcfe98ba, s71\n"
    "s_cmp_eq_u32 s8, 3\n"
    "s_cselect_b32 s70, 0x23670145, s70\n"
    "s_cselect_b32 s71, 0xcdef89ab, s71\n"
    "s_add_u32 s12, s10, 1\n"
    "s_and_b32 s12, s12, 7\n"
    "s_mul_i32 s12, s12, 0x02020202\n"
    "s_and_b32 s13, s70, 0x0f0f0f0f\n"
    "s_add_u32 s13, s13, s12\n"
    "s_and_b32 s13, s13, 0x0f0f0f0f\n"
    "s_lshr_b32 s70, s70, 4\n"
    "s_and_b32 s70, s70, 0x0f0f0f0f\n"
    "s_add_u32 s70, s70, s12\n"
    "s_and_b32 s70, s70, 0x0f0f0f0f\n"
    "s_lshl4_add_u32 s70, s70, s13\n"
    "s_and_b32 s13, s71, 0x0f0f0f0f\n"
    "s_add_u32 s13, s13, s12\n"
    "s_and_b32 s13, s13, 0x0f0f0f0f\n"
    "s_lshr_b32 s71, s71, 4\n"
    "s_and_b32 s71, s71, 0x0f0f0f0f\n"
    "s_add_u32 s71, s71, s12\n"
    "s_and_b32 s71, s71, 0x0f0f0f0f\n"
    "s_lshl4_add_u32 s71, s71, s13\n"
    "v_and_b32 v30, 31, v1\n"
    "v_lshlrev_b32 v30, 3, v30\n"
    "s_lshl2_add_u32 s85, s11, s6\n"
    "s_lshl3_add_u32 s86, s85, s7\n"
    "s_lshl_b32 s85, s85, 16\n"
    "s_lshl_b32 s86, s86, 13\n"
    "s_add_u32 s87, s86, 0x80\n"
    "s_add_u32 s67, s56, 0x4000\n"
    "s_lshl_b32 s84, s6, 21\n"
    "s_lshl_b32 s12, s14, 14\n"
    "s_add_u32 s84, s84, s12\n"
    "s_lshl_b32 s14, s10, 8\n"
    "s_add_u32 s84, s84, s14\n"
    "s_lshl_b32 s12, s6, 16\n"
    "s_sub_u32 s13, s35, s12\n"
    "s_add_u32 s12, s6, 3\n"
    "s_and_b32 s12, s12, 3\n"
    "s_lshl_b32 s12, s12, 16\n"
    "s_add_u32 s12, s12, s13\n"
    "s_add_u32 s89, s12, 0x800\n"
    "s_add_u32 s12, s6, 2\n"
    "s_and_b32 s12, s12, 3\n"
    "s_lshl_b32 s12, s12, 16\n"
    "s_add_u32 s12, s12, s13\n"
    "s_add_u32 s90, s12, 0x1000\n"
    "s_add_u32 s12, s6, 1\n"
    "s_and_b32 s12, s12, 3\n"
    "s_lshl_b32 s12, s12, 16\n"
    "s_add_u32 s12, s12, s13\n"
    "s_add_u32 s91, s12, 0x1800\n"
    "s_lshl_b32 s92, s11, 17\n"
    "s_lshl_b32 s13, s5, 11\n"
    "s_add_u32 s92, s92, s13\n"
    "s_add_u32 s92, s92, s14\n"
    "s_lshl_b32 s13, s6, 6\n"
    "s_add_u32 s92, s92, s13\n"
    "s_add_u32 s93, s92, 0x10000\n"
    "v_lshlrev_b32 v36, 12, v43\n"
    "v_lshl_add_u32 v36, v46, 5, v36\n"
    "v_lshl_add_u32 v36, v48, 4, v36\n"
    "v_add_u32 v37, s56, v20\n"
    "v_add_u32 v38, 0x10400, v37\n"
    "s_mov_b32 s62, 8\n"
    "s_setprio 0\n"
    "s_branch L_la\n"
    "L_compute:\n"
    "s_setprio 1\n"
    "s_lshr_b32 s61, %1, 3\n"
    "s_lshl2_add_u32 s61, s61, s4\n"
    "s_and_b32 s13, s61, 7\n"
    "s_lshr_b32 s61, s61, 3\n"
    "s_and_b32 s61, s61, 15\n"
    "s_lshl1_add_u32 s61, s61, 0\n"
    "s_lshl_b32 m0, s13, 1\n"
    "s_lshl_b32 s12, s13, 2\n"
    "s_lshr_b32 s12, 0x8798896, s12\n"
    "s_and_b32 s12, s12, 15\n"
    "s_add_u32 s12, s12, 9\n"
    "s_lshl_b32 s61, s61, s12\n"
    "s_lshl_b32 s12, s5, 22\n"
    "s_add_u32 s12, s12, 0x100000\n"
    "s_cmp_eq_u32 s13, 6\n"
    "s_cselect_b32 s12, s12, 0\n"
    "s_add_u32 s61, s61, s12\n"
    "s_waitcnt lgkmcnt(0)\n"
    "s_movrels_b64 s[66:67], s[16:17]\n"
    "s_load_dword s60, s[66:67], s61 offset:0x0\n"
    "s_getpc_b64 s[64:65]\n"
    "L_pc:\n"
    "s_sub_u32 s64, s64, L_pc-L_start\n"
    "s_subb_u32 s65, s65, 0\n"
    "s_lshr_b32 s12, %1, 3\n"
    "s_lshl2_add_u32 s12, s12, s4\n"
    "s_lshl_b32 s12, s12, 7\n"
    "s_min_u32 s12, s12, L_exit-L_start-64\n"
    "s_load_dword s62, s[64:65], s12 offset:0x0\n"
    "s_load_dword s63, s[64:65], s12 offset:0x40\n"
    "v_and_b32 v240, 15, v1\n"
    "v_lshrrev_b32 v241, 4, v1\n"
    "v_bfe_u32 v242, v1, 1, 3\n"
    "v_lshrrev_b32 v243, 5, v1\n"
    "v_and_b32 v244, 1, v241\n"
    "s_lshl_b32 s12, s4, 12\n"
    "v_lshl_add_u32 v245, v240, 7, s12\n"
    "v_xor_b32 v246, v241, v242\n"
    "v_lshl_add_u32 v2, v246, 4, v245\n"
    "v_xor_b32 v3, 64, v2\n"
    "v_xor_b32 v246, v243, v242\n"
    "v_lshl_add_u32 v10, v246, 4, v245\n"
    "v_lshl_add_u32 v10, v244, 3, v10\n"
    "v_bfe_u32 v246, v1, 2, 3\n"
    "v_mul_u32_u24 v246, 0x420, v246\n"
    "v_lshl_add_u32 v246, v243, 9, v246\n"
    "v_and_b32 v247, 3, v1\n"
    "v_lshl_add_u32 v246, v247, 3, v246\n"
    "v_add_u32 v4, 0x4000, v246\n"
    "v_lshlrev_b32 v8, 4, v1\n"
    "v_add_u32 v9, s12, v8\n"
    "v_add_u32 v12, 0x10400, v9\n"
    "s_lshl_b32 s12, s7, 13\n"
    "s_sub_u32 s47, s35, s12\n"
    "s_mov_b32 s49, 0x75634210\n"
    "s_cmp_eq_u32 s8, 1\n"
    "s_cselect_b32 s49, 0x75634210, s49\n"
    "s_cmp_eq_u32 s8, 2\n"
    "s_cselect_b32 s49, 0x67451302, s49\n"
    "s_cmp_eq_u32 s8, 3\n"
    "s_cselect_b32 s49, 0x67451302, s49\n"
    "s_and_b32 s12, s8, 2\n"
    "s_sub_u32 s12, s7, s12\n"
    "s_and_b32 s12, s12, 7\n"
    "s_mul_i32 s12, s12, 0x11111111\n"
    "s_add_u32 s49, s49, s12\n"
    "s_and_b32 s49, s49, 0x77777777\n"
    "s_mov_b32 s44, 0\n"
    "s_mov_b32 s45, 0\n"
    "s_mov_b32 s46, s33\n"
    "s_mov_b32 s52, 0x3e0293ee\n"
    "s_mov_b32 s53, 0x3e0293ee\n"
    "s_mov_b32 s51, 0x10400\n"
    "s_add_u32 s54, s35, 0x800\n"
    "s_add_u32 s55, s35, 0x1800\n"
    "v_add_u32 v5, 0, v2\n"
    "v_add_u32 v6, 0, v3\n"
    "v_add_u32 v7, 0, v4\n"
    "v_mov_b64 v[160:161], 0\n"
    "v_mov_b64 v[162:163], 0\n"
    "v_mov_b64 v[164:165], 0\n"
    "v_mov_b64 v[166:167], 0\n"
    "v_mov_b64 v[168:169], 0\n"
    "v_mov_b64 v[170:171], 0\n"
    "v_mov_b64 v[172:173], 0\n"
    "v_mov_b64 v[174:175], 0\n"
    "v_mov_b64 v[176:177], 0\n"
    "v_mov_b64 v[178:179], 0\n"
    "v_mov_b64 v[180:181], 0\n"
    "v_mov_b64 v[182:183], 0\n"
    "v_mov_b64 v[184:185], 0\n"
    "v_mov_b64 v[186:187], 0\n"
    "v_mov_b64 v[188:189], 0\n"
    "v_mov_b64 v[190:191], 0\n"
    "v_mov_b64 v[192:193], 0\n"
    "v_mov_b64 v[194:195], 0\n"
    "v_mov_b64 v[196:197], 0\n"
    "v_mov_b64 v[198:199], 0\n"
    "v_mov_b64 v[200:201], 0\n"
    "v_mov_b64 v[202:203], 0\n"
    "v_mov_b64 v[204:205], 0\n"
    "v_mov_b64 v[206:207], 0\n"
    "v_mov_b64 v[208:209], 0\n"
    "v_mov_b64 v[210:211], 0\n"
    "v_mov_b64 v[212:213], 0\n"
    "v_mov_b64 v[214:215], 0\n"
    "v_mov_b64 v[216:217], 0\n"
    "v_mov_b64 v[218:219], 0\n"
    "v_mov_b64 v[220:221], 0\n"
    "v_mov_b64 v[222:223], 0\n"
    "v_mov_b64 v[144:145], 0\n"
    "v_mov_b64 v[146:147], 0\n"
    "v_mov_b64 v[148:149], 0\n"
    "v_mov_b64 v[150:151], 0\n"
    "v_mov_b64 v[152:153], 0\n"
    "v_mov_b64 v[154:155], 0\n"
    "v_mov_b64 v[156:157], 0\n"
    "v_mov_b64 v[158:159], 0\n"
    "s_waitcnt lgkmcnt(0)\n"
    "s_mov_b32 s36, s28\n"
    "s_and_b32 s37, s29, 0xffff\n"
    "s_mov_b32 s38, 0x1000000\n"
    "s_mov_b32 s39, 0x20000\n"
    "s_barrier\n"
    "ds_read_b128 v[48:51], v5 offset:0\n"
    "ds_read_b128 v[52:55], v5 offset:2048\n"
    "ds_read_b64_tr_b16 v[16:17], v7 offset:0\n"
    "ds_read_b64_tr_b16 v[18:19], v7 offset:256\n"
    "ds_read_b64_tr_b16 v[20:21], v7 offset:32\n"
    "ds_read_b64_tr_b16 v[22:23], v7 offset:288\n"
    "ds_read_b64_tr_b16 v[24:25], v7 offset:64\n"
    "ds_read_b64_tr_b16 v[26:27], v7 offset:320\n"
    "ds_read_b64_tr_b16 v[28:29], v7 offset:96\n"
    "ds_read_b64_tr_b16 v[30:31], v7 offset:352\n"
    "ds_read_b64_tr_b16 v[32:33], v7 offset:128\n"
    "ds_read_b64_tr_b16 v[34:35], v7 offset:384\n"
    "ds_read_b64_tr_b16 v[36:37], v7 offset:160\n"
    "ds_read_b64_tr_b16 v[38:39], v7 offset:416\n"
    "ds_read_b64_tr_b16 v[40:41], v7 offset:192\n"
    "ds_read_b64_tr_b16 v[42:43], v7 offset:448\n"
    "ds_read_b64_tr_b16 v[44:45], v7 offset:224\n"
    "ds_read_b64_tr_b16 v[46:47], v7 offset:480\n"
    "ds_read_b64_tr_b16 v[56:57], v7 offset:8448\n"
    "ds_read_b64_tr_b16 v[58:59], v7 offset:8704\n"
    "ds_read_b128 v[88:91], v6 offset:0\n"
    "s_branch L_c1m\n"
    "L_ck:\n"
    "s_waitcnt lgkmcnt(0)\n"
    "s_barrier\n"
    "v_xor_b32 v240, 0x0, v10\n"
    "v_add_u32 v240, s44, v240\n"
    "v_xor_b32 v241, 0x20, v10\n"
    "v_add_u32 v241, s44, v241\n"
    "v_xor_b32 v242, 0x40, v10\n"
    "v_add_u32 v242, s44, v242\n"
    "v_xor_b32 v243, 0x60, v10\n"
    "v_add_u32 v243, s44, v243\n"
    "ds_read_b64 v[96:97], v240 offset:0\n"
    "ds_read_b64 v[98:99], v241 offset:0\n"
    "ds_read_b64 v[100:101], v240 offset:2048\n"
    "ds_read_b64 v[102:103], v241 offset:2048\n"
    "ds_read_b64 v[104:105], v242 offset:0\n"
    "ds_read_b64 v[106:107], v243 offset:0\n"
    "ds_read_b64 v[108:109], v242 offset:2048\n"
    "ds_read_b64 v[110:111], v243 offset:2048\n"
    "ds_read_b64 v[112:113], v240 offset:16384\n"
    "ds_read_b64 v[114:115], v241 offset:16384\n"
    "ds_read_b64 v[116:117], v240 offset:18432\n"
    "ds_read_b64 v[118:119], v241 offset:18432\n"
    "ds_read_b64 v[120:121], v242 offset:16384\n"
    "ds_read_b64 v[122:123], v243 offset:16384\n"
    "ds_read_b64 v[124:125], v242 offset:18432\n"
    "ds_read_b64 v[126:127], v243 offset:18432\n"
    "s_add_u32 s44, s44, 0x8200\n"
    "s_cmp_eq_u32 s44, 0x20800\n"
    "s_cselect_b32 s44, 0, s44\n"
    "v_add_u32 v5, s44, v2\n"
    "v_add_u32 v6, s44, v3\n"
    "v_add_u32 v7, s44, v4\n"
    "s_mov_b32 s46, 16\n"
    "s_branch L_c1\n"
    "L_c1r:\n"
    "v_mfma_f32_16x16x32_bf16 v[208:211], v[40:43], v[48:51], v[208:211]\n"
    "v_mfma_f32_16x16x32_bf16 v[216:219], v[40:43], v[52:55], v[216:219]\n"
    "v_add_u32 v7, s44, v4\n"
    "v_mfma_f32_16x16x32_bf16 v[220:223], v[44:47], v[52:55], v[220:223]\n"
    "v_mfma_f32_16x16x32_bf16 v[212:215], v[44:47], v[48:51], v[212:215]\n"
    "L_c1:\n"
    "s_waitcnt lgkmcnt(0)\n"
    "s_barrier\n"
    "v_mfma_f32_16x16x32_bf16 v[160:163], v[56:59], v[88:91], v[160:163]\n"
    "ds_read_b128 v[48:51], v5 offset:0\n"
    "ds_read_b128 v[52:55], v5 offset:2048\n"
    "v_mfma_f32_16x16x32_bf16 v[168:171], v[56:59], v[92:95], v[168:171]\n"
    "ds_read_b64_tr_b16 v[16:17], v7 offset:0\n"
    "v_mfma_f32_16x16x32_bf16 v[172:175], v[60:63], v[92:95], v[172:175]\n"
    "ds_read_b64_tr_b16 v[18:19], v7 offset:256\n"
    "v_mfma_f32_16x16x32_bf16 v[164:167], v[60:63], v[88:91], v[164:167]\n"
    "ds_read_b64_tr_b16 v[20:21], v7 offset:32\n"
    "ds_read_b64_tr_b16 v[22:23], v7 offset:288\n"
    "v_mfma_f32_16x16x32_bf16 v[176:179], v[64:67], v[88:91], v[176:179]\n"
    "ds_read_b64_tr_b16 v[24:25], v7 offset:64\n"
    "v_mfma_f32_16x16x32_bf16 v[184:187], v[64:67], v[92:95], v[184:187]\n"
    "ds_read_b64_tr_b16 v[26:27], v7 offset:320\n"
    "v_mfma_f32_16x16x32_bf16 v[188:191], v[68:71], v[92:95], v[188:191]\n"
    "ds_read_b64_tr_b16 v[28:29], v7 offset:96\n"
    "ds_read_b64_tr_b16 v[30:31], v7 offset:352\n"
    "v_mfma_f32_16x16x32_bf16 v[180:183], v[68:71], v[88:91], v[180:183]\n"
    "ds_read_b64_tr_b16 v[32:33], v7 offset:128\n"
    "v_mfma_f32_16x16x32_bf16 v[192:195], v[72:75], v[88:91], v[192:195]\n"
    "ds_read_b64_tr_b16 v[34:35], v7 offset:384\n"
    "v_mfma_f32_16x16x32_bf16 v[200:203], v[72:75], v[92:95], v[200:203]\n"
    "ds_read_b64_tr_b16 v[36:37], v7 offset:160\n"
    "v_mfma_f32_16x16x32_bf16 v[204:207], v[76:79], v[92:95], v[204:207]\n"
    "ds_read_b64_tr_b16 v[38:39], v7 offset:416\n"
    "ds_read_b64_tr_b16 v[40:41], v7 offset:192\n"
    "v_mfma_f32_16x16x32_bf16 v[196:199], v[76:79], v[88:91], v[196:199]\n"
    "ds_read_b64_tr_b16 v[42:43], v7 offset:448\n"
    "v_mfma_f32_16x16x32_bf16 v[208:211], v[80:83], v[88:91], v[208:211]\n"
    "ds_read_b64_tr_b16 v[44:45], v7 offset:224\n"
    "v_mfma_f32_16x16x32_bf16 v[216:219], v[80:83], v[92:95], v[216:219]\n"
    "ds_read_b64_tr_b16 v[46:47], v7 offset:480\n"
    "ds_read_b64_tr_b16 v[56:57], v7 offset:8448\n"
    "v_mfma_f32_16x16x32_bf16 v[220:223], v[84:87], v[92:95], v[220:223]\n"
    "ds_read_b64_tr_b16 v[58:59], v7 offset:8704\n"
    "v_mfma_f32_16x16x32_bf16 v[212:215], v[84:87], v[88:91], v[212:215]\n"
    "ds_read_b128 v[88:91], v6 offset:0\n"
    "L_c1m:\n"
    "s_waitcnt lgkmcnt(11)\n"
    "v_mfma_f32_16x16x32_bf16 v[160:163], v[16:19], v[48:51], v[160:163]\n"
    "ds_read_b128 v[92:95], v6 offset:2048\n"
    "ds_read_b64_tr_b16 v[60:61], v7 offset:8480\n"
    "v_mfma_f32_16x16x32_bf16 v[168:171], v[16:19], v[52:55], v[168:171]\n"
    "ds_read_b64_tr_b16 v[62:63], v7 offset:8736\n"
    "s_add_u32 s44, s44, 0x8200\n"
    "v_mfma_f32_16x16x32_bf16 v[172:175], v[20:23], v[52:55], v[172:175]\n"
    "ds_read_b64_tr_b16 v[64:65], v7 offset:8512\n"
    "s_cmp_eq_u32 s44, 0x20800\n"
    "s_cselect_b32 s44, 0, s44\n"
    "v_mfma_f32_16x16x32_bf16 v[164:167], v[20:23], v[48:51], v[164:167]\n"
    "ds_read_b64_tr_b16 v[66:67], v7 offset:8768\n"
    "s_add_u32 s45, s45, 1\n"
    "v_mfma_f32_16x16x32_bf16 v[176:179], v[24:27], v[48:51], v[176:179]\n"
    "ds_read_b64_tr_b16 v[68:69], v7 offset:8544\n"
    "ds_read_b64_tr_b16 v[70:71], v7 offset:8800\n"
    "v_mfma_f32_16x16x32_bf16 v[184:187], v[24:27], v[52:55], v[184:187]\n"
    "ds_read_b64_tr_b16 v[72:73], v7 offset:8576\n"
    "v_add_u32 v5, s44, v2\n"
    "v_mfma_f32_16x16x32_bf16 v[188:191], v[28:31], v[52:55], v[188:191]\n"
    "ds_read_b64_tr_b16 v[74:75], v7 offset:8832\n"
    "v_add_u32 v6, s44, v3\n"
    "v_mfma_f32_16x16x32_bf16 v[180:183], v[28:31], v[48:51], v[180:183]\n"
    "ds_read_b64_tr_b16 v[76:77], v7 offset:8608\n"
    "s_waitcnt lgkmcnt(13)\n"
    "v_mfma_f32_16x16x32_bf16 v[192:195], v[32:35], v[48:51], v[192:195]\n"
    "ds_read_b64_tr_b16 v[78:79], v7 offset:8864\n"
    "ds_read_b64_tr_b16 v[80:81], v7 offset:8640\n"
    "v_mfma_f32_16x16x32_bf16 v[200:203], v[32:35], v[52:55], v[200:203]\n"
    "ds_read_b64_tr_b16 v[82:83], v7 offset:8896\n"
    "s_cmp_lg_u32 s45, s46\n"
    "v_mfma_f32_16x16x32_bf16 v[204:207], v[36:39], v[52:55], v[204:207]\n"
    "ds_read_b64_tr_b16 v[84:85], v7 offset:8672\n"
    "v_mfma_f32_16x16x32_bf16 v[196:199], v[36:39], v[48:51], v[196:199]\n"
    "ds_read_b64_tr_b16 v[86:87], v7 offset:8928\n"
    "s_cbranch_scc1 L_c1r\n"
    "s_cmp_lg_u32 s45, 16\n"
    "v_mfma_f32_16x16x32_bf16 v[208:211], v[40:43], v[48:51], v[208:211]\n"
    "v_mfma_f32_16x16x32_bf16 v[216:219], v[40:43], v[52:55], v[216:219]\n"
    "v_add_u32 v7, s44, v4\n"
    "v_mfma_f32_16x16x32_bf16 v[220:223], v[44:47], v[52:55], v[220:223]\n"
    "v_mfma_f32_16x16x32_bf16 v[212:215], v[44:47], v[48:51], v[212:215]\n"
    "s_cbranch_scc1 L_ck\n"
    "s_waitcnt lgkmcnt(0)\n"
    "v_mfma_f32_16x16x32_bf16 v[160:163], v[56:59], v[88:91], v[160:163]\n"
    "s_and_b32 s48, s49, 15\n"
    "s_lshr_b32 s49, s49, 4\n"
    "v_mfma_f32_16x16x32_bf16 v[168:171], v[56:59], v[92:95], v[168:171]\n"
    "s_lshl_b32 s48, s48, 13\n"
    "s_add_u32 s48, s48, s47\n"
    "v_mfma_f32_16x16x32_bf16 v[172:175], v[60:63], v[92:95], v[172:175]\n"
    "s_mov_b32 s45, 0\n"
    "v_mfma_f32_16x16x32_bf16 v[164:167], v[60:63], v[88:91], v[164:167]\n"
    "v_add_u32 v5, 0, v2\n"
    "v_mfma_f32_16x16x32_bf16 v[176:179], v[64:67], v[88:91], v[176:179]\n"
    "v_add_u32 v6, 0, v3\n"
    "v_mfma_f32_16x16x32_bf16 v[184:187], v[64:67], v[92:95], v[184:187]\n"
    "v_add_u32 v7, 0, v4\n"
    "v_mfma_f32_16x16x32_bf16 v[188:191], v[68:71], v[92:95], v[188:191]\n"
    "v_mfma_f32_16x16x32_bf16 v[180:183], v[68:71], v[88:91], v[180:183]\n"
    "v_mfma_f32_16x16x32_bf16 v[192:195], v[72:75], v[88:91], v[192:195]\n"
    "v_cvt_pk_bf16_f32 v16, v160, v161\n"
    "v_cvt_pk_bf16_f32 v17, v162, v163\n"
    "v_mfma_f32_16x16x32_bf16 v[200:203], v[72:75], v[92:95], v[200:203]\n"
    "v_cvt_pk_bf16_f32 v18, v164, v165\n"
    "v_cvt_pk_bf16_f32 v19, v166, v167\n"
    "v_mfma_f32_16x16x32_bf16 v[204:207], v[76:79], v[92:95], v[204:207]\n"
    "v_cvt_pk_bf16_f32 v20, v168, v169\n"
    "v_cvt_pk_bf16_f32 v21, v170, v171\n"
    "v_mfma_f32_16x16x32_bf16 v[196:199], v[76:79], v[88:91], v[196:199]\n"
    "v_cvt_pk_bf16_f32 v22, v172, v173\n"
    "v_cvt_pk_bf16_f32 v23, v174, v175\n"
    "v_mfma_f32_16x16x32_bf16 v[208:211], v[80:83], v[88:91], v[208:211]\n"
    "v_cvt_pk_bf16_f32 v24, v176, v177\n"
    "v_cvt_pk_bf16_f32 v25, v178, v179\n"
    "v_mfma_f32_16x16x32_bf16 v[216:219], v[80:83], v[92:95], v[216:219]\n"
    "v_cvt_pk_bf16_f32 v26, v180, v181\n"
    "v_cvt_pk_bf16_f32 v27, v182, v183\n"
    "v_mfma_f32_16x16x32_bf16 v[220:223], v[84:87], v[92:95], v[220:223]\n"
    "v_cvt_pk_bf16_f32 v28, v184, v185\n"
    "v_cvt_pk_bf16_f32 v29, v186, v187\n"
    "v_mfma_f32_16x16x32_bf16 v[212:215], v[84:87], v[88:91], v[212:215]\n"
    "v_cvt_pk_bf16_f32 v30, v188, v189\n"
    "v_cvt_pk_bf16_f32 v31, v190, v191\n"
    "v_mfma_f32_16x16x32_bf16 v[128:131], v[96:99], v[16:19], 0\n"
    "v_cvt_pk_bf16_f32 v32, v192, v193\n"
    "v_cvt_pk_bf16_f32 v33, v194, v195\n"
    "v_mfma_f32_16x16x32_bf16 v[132:135], v[100:103], v[16:19], 0\n"
    "v_cvt_pk_bf16_f32 v34, v196, v197\n"
    "v_cvt_pk_bf16_f32 v35, v198, v199\n"
    "v_mfma_f32_16x16x32_bf16 v[136:139], v[96:99], v[20:23], 0\n"
    "v_cvt_pk_bf16_f32 v36, v200, v201\n"
    "v_cvt_pk_bf16_f32 v37, v202, v203\n"
    "v_mfma_f32_16x16x32_bf16 v[140:143], v[100:103], v[20:23], 0\n"
    "v_cvt_pk_bf16_f32 v38, v204, v205\n"
    "v_cvt_pk_bf16_f32 v39, v206, v207\n"
    "v_mfma_f32_16x16x32_bf16 v[128:131], v[104:107], v[24:27], v[128:131]\n"
    "v_cvt_pk_bf16_f32 v40, v208, v209\n"
    "v_cvt_pk_bf16_f32 v41, v210, v211\n"
    "v_mfma_f32_16x16x32_bf16 v[132:135], v[108:111], v[24:27], v[132:135]\n"
    "v_cvt_pk_bf16_f32 v42, v212, v213\n"
    "v_cvt_pk_bf16_f32 v43, v214, v215\n"
    "v_mfma_f32_16x16x32_bf16 v[136:139], v[104:107], v[28:31], v[136:139]\n"
    "v_cvt_pk_bf16_f32 v44, v216, v217\n"
    "v_cvt_pk_bf16_f32 v45, v218, v219\n"
    "v_mfma_f32_16x16x32_bf16 v[140:143], v[108:111], v[28:31], v[140:143]\n"
    "v_cvt_pk_bf16_f32 v46, v220, v221\n"
    "v_cvt_pk_bf16_f32 v47, v222, v223\n"
    "v_mfma_f32_16x16x32_bf16 v[128:131], v[112:115], v[32:35], v[128:131]\n"
    "v_mov_b64 v[96:97], 0\n"
    "v_mov_b64 v[98:99], 0\n"
    "v_mfma_f32_16x16x32_bf16 v[132:135], v[116:119], v[32:35], v[132:135]\n"
    "v_mov_b64 v[100:101], 0\n"
    "v_mov_b64 v[102:103], 0\n"
    "v_mfma_f32_16x16x32_bf16 v[136:139], v[112:115], v[36:39], v[136:139]\n"
    "v_mov_b64 v[104:105], 0\n"
    "v_mov_b64 v[106:107], 0\n"
    "v_mfma_f32_16x16x32_bf16 v[140:143], v[116:119], v[36:39], v[140:143]\n"
    "v_mov_b64 v[108:109], 0\n"
    "v_mov_b64 v[110:111], 0\n"
    "v_mfma_f32_16x16x32_bf16 v[128:131], v[120:123], v[40:43], v[128:131]\n"
    "v_mov_b64 v[112:113], 0\n"
    "v_mfma_f32_16x16x32_bf16 v[132:135], v[124:127], v[40:43], v[132:135]\n"
    "v_mov_b64 v[114:115], 0\n"
    "v_mfma_f32_16x16x32_bf16 v[136:139], v[120:123], v[44:47], v[136:139]\n"
    "v_mov_b64 v[116:117], 0\n"
    "v_mfma_f32_16x16x32_bf16 v[140:143], v[124:127], v[44:47], v[140:143]\n"
    "v_mov_b64 v[118:119], 0\n"
    "v_mov_b64 v[120:121], 0\n"
    "v_mov_b64 v[122:123], 0\n"
    "v_mov_b64 v[124:125], 0\n"
    "v_mov_b64 v[126:127], 0\n"
    "v_max3_f32 v240, v128, v129, v130\n"
    "v_max3_f32 v242, v131, v132, v133\n"
    "v_max3_f32 v240, v240, v134, v135\n"
    "v_max_f32 v240, v240, v242\n"
    "v_max3_f32 v241, v136, v137, v138\n"
    "v_max3_f32 v243, v139, v140, v141\n"
    "v_max3_f32 v241, v241, v142, v143\n"
    "v_max_f32 v241, v241, v243\n"
    "v_mov_b64 v[242:243], v[240:241]\n"
    "s_nop 1\n"
    "v_permlane16_swap_b32 v240, v242\n"
    "v_permlane16_swap_b32 v241, v243\n"
    "v_max_f32 v240, v240, v242\n"
    "v_max_f32 v241, v241, v243\n"
    "v_mov_b64 v[242:243], v[240:241]\n"
    "s_nop 1\n"
    "v_permlane32_swap_b32 v240, v242\n"
    "v_permlane32_swap_b32 v241, v243\n"
    "v_max_f32 v240, v240, v242\n"
    "v_max_f32 v241, v241, v243\n"
    "v_pk_mul_f32 v[240:241], v[240:241], s[52:53]\n"
    "s_nop 0\n"
    "v_pk_fma_f32 v[128:129], v[128:129], s[52:53], v[240:241] op_sel_hi:[1,1,0] neg_lo:[0,0,1] neg_hi:[0,0,1]\n"
    "v_pk_fma_f32 v[130:131], v[130:131], s[52:53], v[240:241] op_sel_hi:[1,1,0] neg_lo:[0,0,1] neg_hi:[0,0,1]\n"
    "v_pk_fma_f32 v[132:133], v[132:133], s[52:53], v[240:241] op_sel_hi:[1,1,0] neg_lo:[0,0,1] neg_hi:[0,0,1]\n"
    "v_pk_fma_f32 v[134:135], v[134:135], s[52:53], v[240:241] op_sel_hi:[1,1,0] neg_lo:[0,0,1] neg_hi:[0,0,1]\n"
    "v_pk_fma_f32 v[136:137], v[136:137], s[52:53], v[240:241] op_sel:[0,0,1] neg_lo:[0,0,1] neg_hi:[0,0,1]\n"
    "v_pk_fma_f32 v[138:139], v[138:139], s[52:53], v[240:241] op_sel:[0,0,1] neg_lo:[0,0,1] neg_hi:[0,0,1]\n"
    "v_pk_fma_f32 v[140:141], v[140:141], s[52:53], v[240:241] op_sel:[0,0,1] neg_lo:[0,0,1] neg_hi:[0,0,1]\n"
    "v_pk_fma_f32 v[142:143], v[142:143], s[52:53], v[240:241] op_sel:[0,0,1] neg_lo:[0,0,1] neg_hi:[0,0,1]\n"
    "v_exp_f32 v128, v128\n"
    "v_exp_f32 v129, v129\n"
    "v_exp_f32 v130, v130\n"
    "v_exp_f32 v131, v131\n"
    "v_exp_f32 v132, v132\n"
    "v_exp_f32 v133, v133\n"
    "v_exp_f32 v134, v134\n"
    "v_exp_f32 v135, v135\n"
    "v_exp_f32 v136, v136\n"
    "v_exp_f32 v137, v137\n"
    "v_exp_f32 v138, v138\n"
    "v_exp_f32 v139, v139\n"
    "v_exp_f32 v140, v140\n"
    "v_exp_f32 v141, v141\n"
    "v_exp_f32 v142, v142\n"
    "v_exp_f32 v143, v143\n"
    "v_pk_add_f32 v[244:245], v[128:129], v[130:131]\n"
    "v_pk_add_f32 v[248:249], v[136:137], v[138:139]\n"
    "v_pk_add_f32 v[246:247], v[132:133], v[134:135]\n"
    "v_pk_add_f32 v[250:251], v[140:141], v[142:143]\n"
    "v_pk_add_f32 v[244:245], v[244:245], v[246:247]\n"
    "v_pk_add_f32 v[248:249], v[248:249], v[250:251]\n"
    "v_add_f32 v240, v244, v245\n"
    "v_add_f32 v241, v248, v249\n"
    "v_mov_b64 v[242:243], v[240:241]\n"
    "s_nop 1\n"
    "v_permlane16_swap_b32 v240, v242\n"
    "v_permlane16_swap_b32 v241, v243\n"
    "v_pk_add_f32 v[240:241], v[240:241], v[242:243]\n"
    "s_nop 0\n"
    "v_mov_b64 v[242:243], v[240:241]\n"
    "s_nop 1\n"
    "v_permlane32_swap_b32 v240, v242\n"
    "v_permlane32_swap_b32 v241, v243\n"
    "v_pk_add_f32 v[240:241], v[240:241], v[242:243]\n"
    "s_nop 0\n"
    "v_rcp_f32 v240, v240\n"
    "v_rcp_f32 v241, v241\n"
    "s_nop 0\n"
    "v_pk_mul_f32 v[128:129], v[128:129], v[240:241] op_sel_hi:[1,0]\n"
    "v_pk_mul_f32 v[130:131], v[130:131], v[240:241] op_sel_hi:[1,0]\n"
    "v_pk_mul_f32 v[132:133], v[132:133], v[240:241] op_sel_hi:[1,0]\n"
    "v_pk_mul_f32 v[134:135], v[134:135], v[240:241] op_sel_hi:[1,0]\n"
    "v_cvt_pk_bf16_f32 v224, v128, v129\n"
    "v_cvt_pk_bf16_f32 v225, v130, v131\n"
    "v_cvt_pk_bf16_f32 v226, v132, v133\n"
    "v_cvt_pk_bf16_f32 v227, v134, v135\n"
    "v_min_u32 v224, -2, v224\n"
    "v_min_u32 v225, -2, v225\n"
    "v_min_u32 v226, -2, v226\n"
    "v_min_u32 v227, -2, v227\n"
    "buffer_store_dwordx4 v[224:227], v8, s[36:39], s35 offen offset:0 sc1\n"
    "v_pk_mul_f32 v[136:137], v[136:137], v[240:241] op_sel:[0,1]\n"
    "v_pk_mul_f32 v[138:139], v[138:139], v[240:241] op_sel:[0,1]\n"
    "v_pk_mul_f32 v[140:141], v[140:141], v[240:241] op_sel:[0,1]\n"
    "v_pk_mul_f32 v[142:143], v[142:143], v[240:241] op_sel:[0,1]\n"
    "v_cvt_pk_bf16_f32 v228, v136, v137\n"
    "v_cvt_pk_bf16_f32 v229, v138, v139\n"
    "v_cvt_pk_bf16_f32 v230, v140, v141\n"
    "v_cvt_pk_bf16_f32 v231, v142, v143\n"
    "v_min_u32 v228, -2, v228\n"
    "v_min_u32 v229, -2, v229\n"
    "v_min_u32 v230, -2, v230\n"
    "v_min_u32 v231, -2, v231\n"
    "buffer_store_dwordx4 v[228:231], v8, s[36:39], s35 offen offset:1024 sc1\n"
    "s_barrier\n"
    "ds_read_b128 v[48:51], v5 offset:33280\n"
    "ds_read_b128 v[52:55], v5 offset:35328\n"
    "ds_read_b64_tr_b16 v[16:17], v7 offset:33280\n"
    "ds_read_b64_tr_b16 v[18:19], v7 offset:33536\n"
    "ds_read_b64_tr_b16 v[20:21], v7 offset:33312\n"
    "ds_read_b64_tr_b16 v[22:23], v7 offset:33568\n"
    "ds_read_b64_tr_b16 v[24:25], v7 offset:33344\n"
    "ds_read_b64_tr_b16 v[26:27], v7 offset:33600\n"
    "ds_read_b64_tr_b16 v[28:29], v7 offset:33376\n"
    "ds_read_b64_tr_b16 v[30:31], v7 offset:33632\n"
    "ds_read_b64_tr_b16 v[32:33], v7 offset:33408\n"
    "ds_read_b64_tr_b16 v[34:35], v7 offset:33664\n"
    "ds_read_b64_tr_b16 v[36:37], v7 offset:33440\n"
    "ds_read_b64_tr_b16 v[38:39], v7 offset:33696\n"
    "ds_read_b64_tr_b16 v[40:41], v7 offset:33472\n"
    "ds_read_b64_tr_b16 v[42:43], v7 offset:33728\n"
    "ds_read_b64_tr_b16 v[44:45], v7 offset:33504\n"
    "ds_read_b64_tr_b16 v[46:47], v7 offset:33760\n"
    "v_mov_b64 v[128:129], 0\n"
    "v_mov_b64 v[130:131], 0\n"
    "v_mov_b64 v[132:133], 0\n"
    "v_mov_b64 v[134:135], 0\n"
    "v_mov_b64 v[136:137], 0\n"
    "v_mov_b64 v[138:139], 0\n"
    "v_mov_b64 v[140:141], 0\n"
    "v_mov_b64 v[142:143], 0\n"
    "s_waitcnt lgkmcnt(12)\n"
    "v_mfma_f32_16x16x32_bf16 v[160:163], v[48:51], v[16:19], 0\n"
    "ds_read_b128 v[88:91], v6 offset:33280\n"
    "ds_read_b64_tr_b16 v[76:77], v7 offset:41888\n"
    "v_mfma_f32_16x16x32_bf16 v[164:167], v[52:55], v[16:19], 0\n"
    "ds_read_b128 v[92:95], v6 offset:35328\n"
    "ds_read_b64_tr_b16 v[78:79], v7 offset:42144\n"
    "v_mfma_f32_16x16x32_bf16 v[172:175], v[52:55], v[20:23], 0\n"
    "ds_read_b64_tr_b16 v[56:57], v7 offset:41728\n"
    "ds_read_b64_tr_b16 v[80:81], v7 offset:41920\n"
    "v_mfma_f32_16x16x32_bf16 v[168:171], v[48:51], v[20:23], 0\n"
    "ds_read_b64_tr_b16 v[58:59], v7 offset:41984\n"
    "ds_read_b64_tr_b16 v[82:83], v7 offset:42176\n"
    "s_waitcnt lgkmcnt(8)\n"
    "v_mfma_f32_16x16x32_bf16 v[176:179], v[48:51], v[24:27], 0\n"
    "ds_read_b64_tr_b16 v[60:61], v7 offset:41760\n"
    "ds_read_b64_tr_b16 v[84:85], v7 offset:41952\n"
    "v_mfma_f32_16x16x32_bf16 v[180:183], v[52:55], v[24:27], 0\n"
    "ds_read_b64_tr_b16 v[62:63], v7 offset:42016\n"
    "ds_read_b64_tr_b16 v[86:87], v7 offset:42208\n"
    "v_mfma_f32_16x16x32_bf16 v[188:191], v[52:55], v[28:31], 0\n"
    "ds_read_b64_tr_b16 v[64:65], v7 offset:41792\n"
    "v_mov_b64 v[240:241], v[224:225]\n"
    "v_mfma_f32_16x16x32_bf16 v[184:187], v[48:51], v[28:31], 0\n"
    "ds_read_b64_tr_b16 v[66:67], v7 offset:42048\n"
    "v_mov_b64 v[242:243], v[226:227]\n"
    "v_mfma_f32_16x16x32_bf16 v[192:195], v[48:51], v[32:35], 0\n"
    "ds_read_b64_tr_b16 v[68:69], v7 offset:41824\n"
    "v_mov_b64 v[244:245], v[228:229]\n"
    "v_mfma_f32_16x16x32_bf16 v[196:199], v[52:55], v[32:35], 0\n"
    "ds_read_b64_tr_b16 v[70:71], v7 offset:42080\n"
    "v_mov_b64 v[246:247], v[230:231]\n"
    "v_mfma_f32_16x16x32_bf16 v[204:207], v[52:55], v[36:39], 0\n"
    "ds_read_b64_tr_b16 v[72:73], v7 offset:41856\n"
    "v_mfma_f32_16x16x32_bf16 v[200:203], v[48:51], v[36:39], 0\n"
    "ds_read_b64_tr_b16 v[74:75], v7 offset:42112\n"
    "v_mfma_f32_16x16x32_bf16 v[208:211], v[48:51], v[40:43], 0\n"
    "v_add_u32 v7, s51, v7\n"
    "v_mfma_f32_16x16x32_bf16 v[212:215], v[52:55], v[40:43], 0\n"
    "v_add_u32 v5, s51, v5\n"
    "v_mfma_f32_16x16x32_bf16 v[220:223], v[52:55], v[44:47], 0\n"
    "v_add_u32 v6, s51, v6\n"
    "v_mfma_f32_16x16x32_bf16 v[216:219], v[48:51], v[44:47], 0\n"
    "s_sub_u32 s51, 0, s51\n"
    "s_waitcnt lgkmcnt(0)\n"
    "s_barrier\n"
    "ds_read_b64_tr_b16 v[16:17], v7 offset:0\n"
    "ds_read_b64_tr_b16 v[18:19], v7 offset:256\n"
    "ds_read_b64_tr_b16 v[20:21], v7 offset:32\n"
    "ds_read_b64_tr_b16 v[22:23], v7 offset:288\n"
    "ds_read_b64_tr_b16 v[24:25], v7 offset:64\n"
    "ds_read_b64_tr_b16 v[26:27], v7 offset:320\n"
    "ds_read_b64_tr_b16 v[28:29], v7 offset:96\n"
    "ds_read_b64_tr_b16 v[30:31], v7 offset:352\n"
    "s_branch L_c2m\n"
    "L_c2:\n"
    "v_mfma_f32_16x16x32_bf16 v[208:211], v[48:51], v[40:43], v[208:211]\n"
    "v_mfma_f32_16x16x32_bf16 v[212:215], v[52:55], v[40:43], v[212:215]\n"
    "v_mfma_f32_16x16x32_bf16 v[220:223], v[52:55], v[44:47], v[220:223]\n"
    "v_mfma_f32_16x16x32_bf16 v[216:219], v[48:51], v[44:47], v[216:219]\n"
    "s_waitcnt lgkmcnt(0)\n"
    "s_barrier\n"
    "v_mfma_f32_16x16x32_bf16 v[160:163], v[88:91], v[56:59], v[160:163]\n"
    "ds_read_b128 v[48:51], v5 offset:33280\n"
    "s_waitcnt vmcnt(0)\n"
    "v_mfma_f32_16x16x32_bf16 v[164:167], v[92:95], v[56:59], v[164:167]\n"
    "ds_read_b128 v[52:55], v5 offset:35328\n"
    "v_max3_u32 v11, v240, v241, v242\n"
    "v_mfma_f32_16x16x32_bf16 v[172:175], v[92:95], v[60:63], v[172:175]\n"
    "ds_read_b64_tr_b16 v[16:17], v7 offset:33280\n"
    "v_max3_u32 v11, v11, v243, v244\n"
    "v_mfma_f32_16x16x32_bf16 v[168:171], v[88:91], v[60:63], v[168:171]\n"
    "ds_read_b64_tr_b16 v[18:19], v7 offset:33536\n"
    "v_max3_u32 v11, v11, v245, v246\n"
    "v_mfma_f32_16x16x32_bf16 v[176:179], v[88:91], v[64:67], v[176:179]\n"
    "ds_read_b64_tr_b16 v[20:21], v7 offset:33312\n"
    "v_max_u32 v11, v11, v247\n"
    "v_mfma_f32_16x16x32_bf16 v[180:183], v[92:95], v[64:67], v[180:183]\n"
    "ds_read_b64_tr_b16 v[22:23], v7 offset:33568\n"
    "v_cmp_eq_u32 vcc, -1, v11\n"
    "v_mfma_f32_16x16x32_bf16 v[188:191], v[92:95], v[68:71], v[188:191]\n"
    "ds_read_b64_tr_b16 v[24:25], v7 offset:33344\n"
    "v_mov_b64 v[232:233], v[240:241]\n"
    "v_mfma_f32_16x16x32_bf16 v[184:187], v[88:91], v[68:71], v[184:187]\n"
    "ds_read_b64_tr_b16 v[26:27], v7 offset:33600\n"
    "v_mov_b64 v[234:235], v[242:243]\n"
    "v_mfma_f32_16x16x32_bf16 v[192:195], v[88:91], v[72:75], v[192:195]\n"
    "ds_read_b64_tr_b16 v[28:29], v7 offset:33376\n"
    "v_mov_b64 v[236:237], v[244:245]\n"
    "v_mfma_f32_16x16x32_bf16 v[196:199], v[92:95], v[72:75], v[196:199]\n"
    "ds_read_b64_tr_b16 v[30:31], v7 offset:33632\n"
    "v_mov_b64 v[238:239], v[246:247]\n"
    "v_mfma_f32_16x16x32_bf16 v[204:207], v[92:95], v[76:79], v[204:207]\n"
    "ds_read_b64_tr_b16 v[32:33], v7 offset:33408\n"
    "s_waitcnt lgkmcnt(5)\n"
    "s_cbranch_vccnz L_pa_spin\n"
    "L_pa_go:\n"
    "v_mfma_f32_16x16x32_bf16 v[200:203], v[88:91], v[76:79], v[200:203]\n"
    "ds_read_b64_tr_b16 v[34:35], v7 offset:33664\n"
    "v_cvt_pk_bf16_f32 v224, v160, v161\n"
    "v_mfma_f32_16x16x32_bf16 v[208:211], v[88:91], v[80:83], v[208:211]\n"
    "ds_read_b64_tr_b16 v[36:37], v7 offset:33440\n"
    "v_cvt_pk_bf16_f32 v225, v162, v163\n"
    "v_mfma_f32_16x16x32_bf16 v[212:215], v[92:95], v[80:83], v[212:215]\n"
    "ds_read_b64_tr_b16 v[38:39], v7 offset:33696\n"
    "v_cvt_pk_bf16_f32 v226, v164, v165\n"
    "v_mfma_f32_16x16x32_bf16 v[220:223], v[92:95], v[84:87], v[220:223]\n"
    "ds_read_b64_tr_b16 v[40:41], v7 offset:33472\n"
    "v_cvt_pk_bf16_f32 v227, v166, v167\n"
    "v_mfma_f32_16x16x32_bf16 v[216:219], v[88:91], v[84:87], v[216:219]\n"
    "ds_read_b64_tr_b16 v[42:43], v7 offset:33728\n"
    "v_cvt_pk_bf16_f32 v228, v168, v169\n"
    "v_mfma_f32_16x16x32_bf16 v[96:99], v[224:227], v[232:235], v[96:99]\n"
    "ds_read_b64_tr_b16 v[44:45], v7 offset:33504\n"
    "v_cvt_pk_bf16_f32 v229, v170, v171\n"
    "v_mfma_f32_16x16x32_bf16 v[104:107], v[224:227], v[236:239], v[104:107]\n"
    "ds_read_b64_tr_b16 v[46:47], v7 offset:33760\n"
    "v_cvt_pk_bf16_f32 v230, v172, v173\n"
    "v_mfma_f32_16x16x32_bf16 v[160:163], v[48:51], v[16:19], 0\n"
    "ds_read_b128 v[88:91], v6 offset:33280\n"
    "v_cvt_pk_bf16_f32 v231, v174, v175\n"
    "v_mfma_f32_16x16x32_bf16 v[164:167], v[52:55], v[16:19], 0\n"
    "ds_read_b128 v[92:95], v6 offset:35328\n"
    "v_cvt_pk_bf16_f32 v224, v176, v177\n"
    "v_mfma_f32_16x16x32_bf16 v[100:103], v[228:231], v[232:235], v[100:103]\n"
    "ds_read_b64_tr_b16 v[56:57], v7 offset:41728\n"
    "v_cvt_pk_bf16_f32 v225, v178, v179\n"
    "v_mfma_f32_16x16x32_bf16 v[108:111], v[228:231], v[236:239], v[108:111]\n"
    "ds_read_b64_tr_b16 v[58:59], v7 offset:41984\n"
    "v_cvt_pk_bf16_f32 v226, v180, v181\n"
    "v_mfma_f32_16x16x32_bf16 v[172:175], v[52:55], v[20:23], 0\n"
    "ds_read_b64_tr_b16 v[60:61], v7 offset:41760\n"
    "v_cvt_pk_bf16_f32 v227, v182, v183\n"
    "v_mfma_f32_16x16x32_bf16 v[168:171], v[48:51], v[20:23], 0\n"
    "ds_read_b64_tr_b16 v[62:63], v7 offset:42016\n"
    "v_cvt_pk_bf16_f32 v228, v184, v185\n"
    "v_mfma_f32_16x16x32_bf16 v[112:115], v[224:227], v[232:235], v[112:115]\n"
    "ds_read_b64_tr_b16 v[64:65], v7 offset:41792\n"
    "v_cvt_pk_bf16_f32 v229, v186, v187\n"
    "v_mfma_f32_16x16x32_bf16 v[120:123], v[224:227], v[236:239], v[120:123]\n"
    "ds_read_b64_tr_b16 v[66:67], v7 offset:42048\n"
    "v_cvt_pk_bf16_f32 v230, v188, v189\n"
    "s_waitcnt lgkmcnt(12)\n"
    "v_mfma_f32_16x16x32_bf16 v[176:179], v[48:51], v[24:27], 0\n"
    "ds_read_b64_tr_b16 v[68:69], v7 offset:41824\n"
    "v_cvt_pk_bf16_f32 v231, v190, v191\n"
    "v_mfma_f32_16x16x32_bf16 v[180:183], v[52:55], v[24:27], 0\n"
    "ds_read_b64_tr_b16 v[70:71], v7 offset:42080\n"
    "v_cvt_pk_bf16_f32 v224, v192, v193\n"
    "v_mfma_f32_16x16x32_bf16 v[116:119], v[228:231], v[232:235], v[116:119]\n"
    "ds_read_b64_tr_b16 v[72:73], v7 offset:41856\n"
    "v_cvt_pk_bf16_f32 v225, v194, v195\n"
    "v_mfma_f32_16x16x32_bf16 v[124:127], v[228:231], v[236:239], v[124:127]\n"
    "ds_read_b64_tr_b16 v[74:75], v7 offset:42112\n"
    "v_cvt_pk_bf16_f32 v226, v196, v197\n"
    "v_mfma_f32_16x16x32_bf16 v[188:191], v[52:55], v[28:31], 0\n"
    "ds_read_b64_tr_b16 v[76:77], v7 offset:41888\n"
    "v_cvt_pk_bf16_f32 v227, v198, v199\n"
    "v_mfma_f32_16x16x32_bf16 v[184:187], v[48:51], v[28:31], 0\n"
    "ds_read_b64_tr_b16 v[78:79], v7 offset:42144\n"
    "v_cvt_pk_bf16_f32 v228, v200, v201\n"
    "v_mfma_f32_16x16x32_bf16 v[128:131], v[224:227], v[232:235], v[128:131]\n"
    "ds_read_b64_tr_b16 v[80:81], v7 offset:41920\n"
    "v_cvt_pk_bf16_f32 v229, v202, v203\n"
    "v_mfma_f32_16x16x32_bf16 v[136:139], v[224:227], v[236:239], v[136:139]\n"
    "ds_read_b64_tr_b16 v[82:83], v7 offset:42176\n"
    "v_cvt_pk_bf16_f32 v230, v204, v205\n"
    "v_mfma_f32_16x16x32_bf16 v[192:195], v[48:51], v[32:35], 0\n"
    "ds_read_b64_tr_b16 v[84:85], v7 offset:41952\n"
    "v_cvt_pk_bf16_f32 v231, v206, v207\n"
    "v_mfma_f32_16x16x32_bf16 v[196:199], v[52:55], v[32:35], 0\n"
    "ds_read_b64_tr_b16 v[86:87], v7 offset:42208\n"
    "v_cvt_pk_bf16_f32 v224, v208, v209\n"
    "v_mfma_f32_16x16x32_bf16 v[132:135], v[228:231], v[232:235], v[132:135]\n"
    "v_cvt_pk_bf16_f32 v225, v210, v211\n"
    "v_add_u32 v7, s51, v7\n"
    "v_mfma_f32_16x16x32_bf16 v[140:143], v[228:231], v[236:239], v[140:143]\n"
    "v_cvt_pk_bf16_f32 v226, v212, v213\n"
    "v_add_u32 v5, s51, v5\n"
    "buffer_load_dwordx4 v[240:243], v8, s[36:39], s50 offen offset:0 sc1\n"
    "v_mfma_f32_16x16x32_bf16 v[204:207], v[52:55], v[36:39], 0\n"
    "v_cvt_pk_bf16_f32 v227, v214, v215\n"
    "v_add_u32 v6, s51, v6\n"
    "buffer_load_dwordx4 v[244:247], v8, s[36:39], s50 offen offset:1024 sc1\n"
    "v_mfma_f32_16x16x32_bf16 v[200:203], v[48:51], v[36:39], 0\n"
    "v_cvt_pk_bf16_f32 v228, v216, v217\n"
    "s_waitcnt lgkmcnt(0)\n"
    "s_barrier\n"
    "v_mfma_f32_16x16x32_bf16 v[144:147], v[224:227], v[232:235], v[144:147]\n"
    "ds_read_b64_tr_b16 v[16:17], v7 offset:0\n"
    "v_cvt_pk_bf16_f32 v229, v218, v219\n"
    "v_mfma_f32_16x16x32_bf16 v[152:155], v[224:227], v[236:239], v[152:155]\n"
    "ds_read_b64_tr_b16 v[18:19], v7 offset:256\n"
    "v_cvt_pk_bf16_f32 v230, v220, v221\n"
    "v_mfma_f32_16x16x32_bf16 v[208:211], v[48:51], v[40:43], 0\n"
    "ds_read_b64_tr_b16 v[20:21], v7 offset:32\n"
    "v_cvt_pk_bf16_f32 v231, v222, v223\n"
    "v_mfma_f32_16x16x32_bf16 v[212:215], v[52:55], v[40:43], 0\n"
    "ds_read_b64_tr_b16 v[22:23], v7 offset:288\n"
    "s_sub_u32 s51, 0, s51\n"
    "v_mfma_f32_16x16x32_bf16 v[148:151], v[228:231], v[232:235], v[148:151]\n"
    "ds_read_b64_tr_b16 v[24:25], v7 offset:64\n"
    "s_mov_b32 s48, s50\n"
    "v_mfma_f32_16x16x32_bf16 v[156:159], v[228:231], v[236:239], v[156:159]\n"
    "ds_read_b64_tr_b16 v[26:27], v7 offset:320\n"
    "v_mfma_f32_16x16x32_bf16 v[220:223], v[52:55], v[44:47], 0\n"
    "ds_read_b64_tr_b16 v[28:29], v7 offset:96\n"
    "v_mfma_f32_16x16x32_bf16 v[216:219], v[48:51], v[44:47], 0\n"
    "ds_read_b64_tr_b16 v[30:31], v7 offset:352\n"
    "L_c2m:\n"
    "v_mfma_f32_16x16x32_bf16 v[160:163], v[88:91], v[56:59], v[160:163]\n"
    "ds_read_b128 v[48:51], v5 offset:0\n"
    "v_mfma_f32_16x16x32_bf16 v[164:167], v[92:95], v[56:59], v[164:167]\n"
    "ds_read_b128 v[52:55], v5 offset:2048\n"
    "v_mfma_f32_16x16x32_bf16 v[172:175], v[92:95], v[60:63], v[172:175]\n"
    "ds_read_b64_tr_b16 v[32:33], v7 offset:128\n"
    "s_add_u32 s45, s45, 1\n"
    "v_mfma_f32_16x16x32_bf16 v[168:171], v[88:91], v[60:63], v[168:171]\n"
    "ds_read_b64_tr_b16 v[34:35], v7 offset:384\n"
    "v_mfma_f32_16x16x32_bf16 v[176:179], v[88:91], v[64:67], v[176:179]\n"
    "ds_read_b64_tr_b16 v[36:37], v7 offset:160\n"
    "s_and_b32 s50, s49, 15\n"
    "v_mfma_f32_16x16x32_bf16 v[180:183], v[92:95], v[64:67], v[180:183]\n"
    "ds_read_b64_tr_b16 v[38:39], v7 offset:416\n"
    "s_lshr_b32 s49, s49, 4\n"
    "v_mfma_f32_16x16x32_bf16 v[188:191], v[92:95], v[68:71], v[188:191]\n"
    "ds_read_b64_tr_b16 v[40:41], v7 offset:192\n"
    "s_lshl_b32 s50, s50, 13\n"
    "v_mfma_f32_16x16x32_bf16 v[184:187], v[88:91], v[68:71], v[184:187]\n"
    "ds_read_b64_tr_b16 v[42:43], v7 offset:448\n"
    "s_add_u32 s50, s50, s47\n"
    "v_mfma_f32_16x16x32_bf16 v[192:195], v[88:91], v[72:75], v[192:195]\n"
    "ds_read_b64_tr_b16 v[44:45], v7 offset:224\n"
    "v_mfma_f32_16x16x32_bf16 v[196:199], v[92:95], v[72:75], v[196:199]\n"
    "ds_read_b64_tr_b16 v[46:47], v7 offset:480\n"
    "v_mfma_f32_16x16x32_bf16 v[204:207], v[92:95], v[76:79], v[204:207]\n"
    "ds_read_b64_tr_b16 v[56:57], v7 offset:8448\n"
    "v_mfma_f32_16x16x32_bf16 v[200:203], v[88:91], v[76:79], v[200:203]\n"
    "ds_read_b64_tr_b16 v[58:59], v7 offset:8704\n"
    "v_mfma_f32_16x16x32_bf16 v[208:211], v[88:91], v[80:83], v[208:211]\n"
    "ds_read_b64_tr_b16 v[60:61], v7 offset:8480\n"
    "v_mfma_f32_16x16x32_bf16 v[212:215], v[92:95], v[80:83], v[212:215]\n"
    "ds_read_b64_tr_b16 v[62:63], v7 offset:8736\n"
    "v_mfma_f32_16x16x32_bf16 v[220:223], v[92:95], v[84:87], v[220:223]\n"
    "ds_read_b64_tr_b16 v[64:65], v7 offset:8512\n"
    "v_mfma_f32_16x16x32_bf16 v[216:219], v[88:91], v[84:87], v[216:219]\n"
    "ds_read_b64_tr_b16 v[66:67], v7 offset:8768\n"
    "s_waitcnt lgkmcnt(6)\n"
    "v_mfma_f32_16x16x32_bf16 v[160:163], v[48:51], v[16:19], v[160:163]\n"
    "ds_read_b128 v[88:91], v6 offset:0\n"
    "v_mfma_f32_16x16x32_bf16 v[164:167], v[52:55], v[16:19], v[164:167]\n"
    "ds_read_b128 v[92:95], v6 offset:2048\n"
    "v_mfma_f32_16x16x32_bf16 v[172:175], v[52:55], v[20:23], v[172:175]\n"
    "ds_read_b64_tr_b16 v[68:69], v7 offset:8544\n"
    "v_mfma_f32_16x16x32_bf16 v[168:171], v[48:51], v[20:23], v[168:171]\n"
    "ds_read_b64_tr_b16 v[70:71], v7 offset:8800\n"
    "v_mfma_f32_16x16x32_bf16 v[176:179], v[48:51], v[24:27], v[176:179]\n"
    "ds_read_b64_tr_b16 v[72:73], v7 offset:8576\n"
    "v_mfma_f32_16x16x32_bf16 v[180:183], v[52:55], v[24:27], v[180:183]\n"
    "ds_read_b64_tr_b16 v[74:75], v7 offset:8832\n"
    "v_mfma_f32_16x16x32_bf16 v[188:191], v[52:55], v[28:31], v[188:191]\n"
    "ds_read_b64_tr_b16 v[76:77], v7 offset:8608\n"
    "v_mfma_f32_16x16x32_bf16 v[184:187], v[48:51], v[28:31], v[184:187]\n"
    "ds_read_b64_tr_b16 v[78:79], v7 offset:8864\n"
    "v_mfma_f32_16x16x32_bf16 v[192:195], v[48:51], v[32:35], v[192:195]\n"
    "ds_read_b64_tr_b16 v[80:81], v7 offset:8640\n"
    "v_mfma_f32_16x16x32_bf16 v[196:199], v[52:55], v[32:35], v[196:199]\n"
    "ds_read_b64_tr_b16 v[82:83], v7 offset:8896\n"
    "v_mfma_f32_16x16x32_bf16 v[204:207], v[52:55], v[36:39], v[204:207]\n"
    "ds_read_b64_tr_b16 v[84:85], v7 offset:8672\n"
    "s_cmp_lt_u32 s45, 6\n"
    "v_mfma_f32_16x16x32_bf16 v[200:203], v[48:51], v[36:39], v[200:203]\n"
    "ds_read_b64_tr_b16 v[86:87], v7 offset:8928\n"
    "s_cbranch_scc1 L_c2\n"
    "L_fin:\n"
    "v_mfma_f32_16x16x32_bf16 v[208:211], v[48:51], v[40:43], v[208:211]\n"
    "buffer_load_dwordx4 v[232:235], v8, s[36:39], s50 offen offset:0 sc1\n"
    "v_mfma_f32_16x16x32_bf16 v[212:215], v[52:55], v[40:43], v[212:215]\n"
    "v_mfma_f32_16x16x32_bf16 v[220:223], v[52:55], v[44:47], v[220:223]\n"
    "buffer_load_dwordx4 v[236:239], v8, s[36:39], s50 offen offset:1024 sc1\n"
    "v_mfma_f32_16x16x32_bf16 v[216:219], v[48:51], v[44:47], v[216:219]\n"
    "s_waitcnt lgkmcnt(0)\n"
    "s_barrier\n"
    "v_mfma_f32_16x16x32_bf16 v[160:163], v[88:91], v[56:59], v[160:163]\n"
    "ds_read_b128 v[16:19], v2 offset:33280\n"
    "s_waitcnt vmcnt(2)\n"
    "v_mfma_f32_16x16x32_bf16 v[164:167], v[92:95], v[56:59], v[164:167]\n"
    "ds_read_b128 v[20:23], v2 offset:35328\n"
    "v_max3_u32 v11, v240, v241, v242\n"
    "v_mfma_f32_16x16x32_bf16 v[172:175], v[92:95], v[60:63], v[172:175]\n"
    "ds_read_b128 v[24:27], v3 offset:33280\n"
    "v_max3_u32 v11, v11, v243, v244\n"
    "v_mfma_f32_16x16x32_bf16 v[168:171], v[88:91], v[60:63], v[168:171]\n"
    "ds_read_b128 v[28:31], v3 offset:35328\n"
    "v_max3_u32 v11, v11, v245, v246\n"
    "v_mfma_f32_16x16x32_bf16 v[176:179], v[88:91], v[64:67], v[176:179]\n"
    "ds_read_b64_tr_b16 v[56:57], v4 offset:33280\n"
    "v_max_u32 v11, v11, v247\n"
    "v_mfma_f32_16x16x32_bf16 v[180:183], v[92:95], v[64:67], v[180:183]\n"
    "ds_read_b64_tr_b16 v[58:59], v4 offset:33536\n"
    "v_cmp_eq_u32 vcc, -1, v11\n"
    "v_mfma_f32_16x16x32_bf16 v[188:191], v[92:95], v[68:71], v[188:191]\n"
    "ds_read_b64_tr_b16 v[60:61], v4 offset:33312\n"
    "v_mfma_f32_16x16x32_bf16 v[184:187], v[88:91], v[68:71], v[184:187]\n"
    "ds_read_b64_tr_b16 v[62:63], v4 offset:33568\n"
    "s_cbranch_vccnz L_pb_spin\n"
    "L_pb_go:\n"
    "v_mfma_f32_16x16x32_bf16 v[192:195], v[88:91], v[72:75], v[192:195]\n"
    "ds_read_b64_tr_b16 v[64:65], v4 offset:41728\n"
    "s_and_b32 s48, s49, 15\n"
    "v_mfma_f32_16x16x32_bf16 v[196:199], v[92:95], v[72:75], v[196:199]\n"
    "ds_read_b64_tr_b16 v[66:67], v4 offset:41984\n"
    "s_lshr_b32 s49, s49, 4\n"
    "v_mfma_f32_16x16x32_bf16 v[204:207], v[92:95], v[76:79], v[204:207]\n"
    "ds_read_b64_tr_b16 v[68:69], v4 offset:41760\n"
    "s_lshl_b32 s48, s48, 13\n"
    "v_mfma_f32_16x16x32_bf16 v[200:203], v[88:91], v[76:79], v[200:203]\n"
    "ds_read_b64_tr_b16 v[70:71], v4 offset:42016\n"
    "v_cvt_pk_bf16_f32 v224, v160, v161\n"
    "v_mfma_f32_16x16x32_bf16 v[208:211], v[88:91], v[80:83], v[208:211]\n"
    "v_cvt_pk_bf16_f32 v225, v162, v163\n"
    "s_add_u32 s48, s48, s47\n"
    "v_mfma_f32_16x16x32_bf16 v[212:215], v[92:95], v[80:83], v[212:215]\n"
    "v_cvt_pk_bf16_f32 v226, v164, v165\n"
    "v_add_u32 v5, 0x10400, v2\n"
    "v_mfma_f32_16x16x32_bf16 v[220:223], v[92:95], v[84:87], v[220:223]\n"
    "v_cvt_pk_bf16_f32 v227, v166, v167\n"
    "v_add_u32 v6, 0x10400, v3\n"
    "v_mfma_f32_16x16x32_bf16 v[216:219], v[88:91], v[84:87], v[216:219]\n"
    "v_cvt_pk_bf16_f32 v228, v168, v169\n"
    "v_add_u32 v7, 0x10400, v4\n"
    "v_mfma_f32_16x16x32_bf16 v[96:99], v[224:227], v[240:243], v[96:99]\n"
    "v_cvt_pk_bf16_f32 v229, v170, v171\n"
    "s_waitcnt lgkmcnt(0)\n"
    "v_mfma_f32_16x16x32_bf16 v[104:107], v[224:227], v[244:247], v[104:107]\n"
    "v_cvt_pk_bf16_f32 v230, v172, v173\n"
    "v_mfma_f32_16x16x32_bf16 v[48:51], v[16:19], v[56:59], 0\n"
    "v_cvt_pk_bf16_f32 v231, v174, v175\n"
    "v_mfma_f32_16x16x32_bf16 v[52:55], v[20:23], v[56:59], 0\n"
    "v_cvt_pk_bf16_f32 v224, v176, v177\n"
    "s_barrier\n"
    "v_mfma_f32_16x16x32_bf16 v[100:103], v[228:231], v[240:243], v[100:103]\n"
    "ds_read_b128 v[32:35], v5 offset:0\n"
    "v_cvt_pk_bf16_f32 v225, v178, v179\n"
    "v_mfma_f32_16x16x32_bf16 v[108:111], v[228:231], v[244:247], v[108:111]\n"
    "ds_read_b128 v[36:39], v5 offset:2048\n"
    "v_cvt_pk_bf16_f32 v226, v180, v181\n"
    "v_mfma_f32_16x16x32_bf16 v[92:95], v[20:23], v[60:63], 0\n"
    "ds_read_b64_tr_b16 v[72:73], v7 offset:0\n"
    "v_cvt_pk_bf16_f32 v227, v182, v183\n"
    "v_mfma_f32_16x16x32_bf16 v[88:91], v[16:19], v[60:63], 0\n"
    "ds_read_b64_tr_b16 v[74:75], v7 offset:256\n"
    "v_cvt_pk_bf16_f32 v228, v184, v185\n"
    "v_mfma_f32_16x16x32_bf16 v[112:115], v[224:227], v[240:243], v[112:115]\n"
    "ds_read_b64_tr_b16 v[76:77], v7 offset:32\n"
    "v_cvt_pk_bf16_f32 v229, v186, v187\n"
    "v_mfma_f32_16x16x32_bf16 v[120:123], v[224:227], v[244:247], v[120:123]\n"
    "ds_read_b64_tr_b16 v[78:79], v7 offset:288\n"
    "v_cvt_pk_bf16_f32 v230, v188, v189\n"
    "v_mfma_f32_16x16x32_bf16 v[48:51], v[24:27], v[64:67], v[48:51]\n"
    "ds_read_b128 v[40:43], v6 offset:0\n"
    "v_cvt_pk_bf16_f32 v231, v190, v191\n"
    "v_mfma_f32_16x16x32_bf16 v[52:55], v[28:31], v[64:67], v[52:55]\n"
    "ds_read_b128 v[44:47], v6 offset:2048\n"
    "v_cvt_pk_bf16_f32 v224, v192, v193\n"
    "v_mfma_f32_16x16x32_bf16 v[116:119], v[228:231], v[240:243], v[116:119]\n"
    "ds_read_b64_tr_b16 v[80:81], v7 offset:8448\n"
    "v_cvt_pk_bf16_f32 v225, v194, v195\n"
    "v_mfma_f32_16x16x32_bf16 v[124:127], v[228:231], v[244:247], v[124:127]\n"
    "ds_read_b64_tr_b16 v[82:83], v7 offset:8704\n"
    "v_cvt_pk_bf16_f32 v226, v196, v197\n"
    "v_mfma_f32_16x16x32_bf16 v[92:95], v[28:31], v[68:71], v[92:95]\n"
    "ds_read_b64_tr_b16 v[84:85], v7 offset:8480\n"
    "v_cvt_pk_bf16_f32 v227, v198, v199\n"
    "v_mfma_f32_16x16x32_bf16 v[88:91], v[24:27], v[68:71], v[88:91]\n"
    "ds_read_b64_tr_b16 v[86:87], v7 offset:8736\n"
    "v_cvt_pk_bf16_f32 v228, v200, v201\n"
    "v_mfma_f32_16x16x32_bf16 v[128:131], v[224:227], v[240:243], v[128:131]\n"
    "ds_read_b64_tr_b16 v[56:57], v4 offset:33344\n"
    "v_cvt_pk_bf16_f32 v229, v202, v203\n"
    "v_mfma_f32_16x16x32_bf16 v[136:139], v[224:227], v[244:247], v[136:139]\n"
    "ds_read_b64_tr_b16 v[58:59], v4 offset:33600\n"
    "v_cvt_pk_bf16_f32 v230, v204, v205\n"
    "s_waitcnt lgkmcnt(8)\n"
    "v_mfma_f32_16x16x32_bf16 v[48:51], v[32:35], v[72:75], v[48:51]\n"
    "ds_read_b64_tr_b16 v[60:61], v4 offset:33376\n"
    "v_cvt_pk_bf16_f32 v231, v206, v207\n"
    "v_mfma_f32_16x16x32_bf16 v[52:55], v[36:39], v[72:75], v[52:55]\n"
    "ds_read_b64_tr_b16 v[62:63], v4 offset:33632\n"
    "v_cvt_pk_bf16_f32 v224, v208, v209\n"
    "v_mfma_f32_16x16x32_bf16 v[132:135], v[228:231], v[240:243], v[132:135]\n"
    "ds_read_b64_tr_b16 v[64:65], v4 offset:41792\n"
    "v_cvt_pk_bf16_f32 v225, v210, v211\n"
    "v_mfma_f32_16x16x32_bf16 v[140:143], v[228:231], v[244:247], v[140:143]\n"
    "ds_read_b64_tr_b16 v[66:67], v4 offset:42048\n"
    "v_cvt_pk_bf16_f32 v226, v212, v213\n"
    "v_mfma_f32_16x16x32_bf16 v[92:95], v[36:39], v[76:79], v[92:95]\n"
    "ds_read_b64_tr_b16 v[68:69], v4 offset:41824\n"
    "v_cvt_pk_bf16_f32 v227, v214, v215\n"
    "v_mfma_f32_16x16x32_bf16 v[88:91], v[32:35], v[76:79], v[88:91]\n"
    "ds_read_b64_tr_b16 v[70:71], v4 offset:42080\n"
    "v_cvt_pk_bf16_f32 v228, v216, v217\n"
    "v_mfma_f32_16x16x32_bf16 v[144:147], v[224:227], v[240:243], v[144:147]\n"
    "ds_read_b64_tr_b16 v[72:73], v7 offset:64\n"
    "v_cvt_pk_bf16_f32 v229, v218, v219\n"
    "v_mfma_f32_16x16x32_bf16 v[152:155], v[224:227], v[244:247], v[152:155]\n"
    "ds_read_b64_tr_b16 v[74:75], v7 offset:320\n"
    "v_cvt_pk_bf16_f32 v230, v220, v221\n"
    "s_waitcnt lgkmcnt(10)\n"
    "v_mfma_f32_16x16x32_bf16 v[48:51], v[40:43], v[80:83], v[48:51]\n"
    "ds_read_b64_tr_b16 v[76:77], v7 offset:96\n"
    "v_cvt_pk_bf16_f32 v231, v222, v223\n"
    "v_mfma_f32_16x16x32_bf16 v[52:55], v[44:47], v[80:83], v[52:55]\n"
    "ds_read_b64_tr_b16 v[78:79], v7 offset:352\n"
    "s_waitcnt vmcnt(0)\n"
    "v_mfma_f32_16x16x32_bf16 v[148:151], v[228:231], v[240:243], v[148:151]\n"
    "ds_read_b64_tr_b16 v[80:81], v7 offset:8512\n"
    "v_max3_u32 v11, v232, v233, v234\n"
    "v_mfma_f32_16x16x32_bf16 v[156:159], v[228:231], v[244:247], v[156:159]\n"
    "ds_read_b64_tr_b16 v[82:83], v7 offset:8768\n"
    "v_max3_u32 v11, v11, v235, v236\n"
    "v_mfma_f32_16x16x32_bf16 v[92:95], v[44:47], v[84:87], v[92:95]\n"
    "s_barrier\n"
    "v_max3_u32 v11, v11, v237, v238\n"
    "v_mfma_f32_16x16x32_bf16 v[88:91], v[40:43], v[84:87], v[88:91]\n"
    "ds_read_b64_tr_b16 v[84:85], v7 offset:8544\n"
    "s_waitcnt lgkmcnt(7)\n"
    "v_max_u32 v11, v11, v239\n"
    "v_mfma_f32_16x16x32_bf16 v[160:163], v[16:19], v[56:59], 0\n"
    "ds_read_b64_tr_b16 v[86:87], v7 offset:8800\n"
    "v_cmp_eq_u32 vcc, -1, v11\n"
    "v_mfma_f32_16x16x32_bf16 v[164:167], v[20:23], v[56:59], 0\n"
    "ds_read_b64_tr_b16 v[56:57], v7 offset:33280\n"
    "v_mfma_f32_16x16x32_bf16 v[172:175], v[20:23], v[60:63], 0\n"
    "ds_read_b64_tr_b16 v[58:59], v7 offset:33536\n"
    "s_cbranch_vccnz L_pf0_spin\n"
    "L_pf0_go:\n"
    "v_mfma_f32_16x16x32_bf16 v[168:171], v[16:19], v[60:63], 0\n"
    "ds_read_b64_tr_b16 v[60:61], v7 offset:33312\n"
    "v_cvt_pk_bf16_f32 v224, v48, v49\n"
    "v_mfma_f32_16x16x32_bf16 v[160:163], v[24:27], v[64:67], v[160:163]\n"
    "ds_read_b64_tr_b16 v[62:63], v7 offset:33568\n"
    "v_cvt_pk_bf16_f32 v225, v50, v51\n"
    "v_mfma_f32_16x16x32_bf16 v[164:167], v[28:31], v[64:67], v[164:167]\n"
    "ds_read_b64_tr_b16 v[64:65], v7 offset:41728\n"
    "v_cvt_pk_bf16_f32 v226, v52, v53\n"
    "v_mfma_f32_16x16x32_bf16 v[172:175], v[28:31], v[68:71], v[172:175]\n"
    "ds_read_b64_tr_b16 v[66:67], v7 offset:41984\n"
    "v_cvt_pk_bf16_f32 v227, v54, v55\n"
    "v_mfma_f32_16x16x32_bf16 v[168:171], v[24:27], v[68:71], v[168:171]\n"
    "ds_read_b64_tr_b16 v[68:69], v7 offset:41760\n"
    "v_cvt_pk_bf16_f32 v228, v88, v89\n"
    "v_mfma_f32_16x16x32_bf16 v[96:99], v[224:227], v[232:235], v[96:99]\n"
    "ds_read_b64_tr_b16 v[70:71], v7 offset:42016\n"
    "v_cvt_pk_bf16_f32 v229, v90, v91\n"
    "v_mfma_f32_16x16x32_bf16 v[104:107], v[224:227], v[236:239], v[104:107]\n"
    "ds_read_b128 v[192:195], v5 offset:33280\n"
    "v_cvt_pk_bf16_f32 v230, v92, v93\n"
    "s_waitcnt lgkmcnt(9)\n"
    "v_mfma_f32_16x16x32_bf16 v[160:163], v[32:35], v[72:75], v[160:163]\n"
    "ds_read_b128 v[196:199], v5 offset:35328\n"
    "v_cvt_pk_bf16_f32 v231, v94, v95\n"
    "s_barrier\n"
    "v_mfma_f32_16x16x32_bf16 v[164:167], v[36:39], v[72:75], v[164:167]\n"
    "ds_read_b64_tr_b16 v[72:73], v4 offset:0\n"
    "v_mfma_f32_16x16x32_bf16 v[100:103], v[228:231], v[232:235], v[100:103]\n"
    "ds_read_b64_tr_b16 v[74:75], v4 offset:256\n"
    "v_mfma_f32_16x16x32_bf16 v[108:111], v[228:231], v[236:239], v[108:111]\n"
    "ds_read_b128 v[200:203], v6 offset:33280\n"
    "buffer_load_dwordx4 v[248:251], v8, s[36:39], s48 offen offset:0 sc1\n"
    "v_mfma_f32_16x16x32_bf16 v[172:175], v[36:39], v[76:79], v[172:175]\n"
    "ds_read_b128 v[204:207], v6 offset:35328\n"
    "buffer_load_dwordx4 v[252:255], v8, s[36:39], s48 offen offset:1024 sc1\n"
    "v_mfma_f32_16x16x32_bf16 v[168:171], v[32:35], v[76:79], v[168:171]\n"
    "ds_read_b64_tr_b16 v[76:77], v4 offset:32\n"
    "v_mfma_f32_16x16x32_bf16 v[160:163], v[40:43], v[80:83], v[160:163]\n"
    "ds_read_b64_tr_b16 v[78:79], v4 offset:288\n"
    "v_mfma_f32_16x16x32_bf16 v[164:167], v[44:47], v[80:83], v[164:167]\n"
    "ds_read_b64_tr_b16 v[80:81], v4 offset:8448\n"
    "v_mfma_f32_16x16x32_bf16 v[172:175], v[44:47], v[84:87], v[172:175]\n"
    "ds_read_b64_tr_b16 v[82:83], v4 offset:8704\n"
    "v_mfma_f32_16x16x32_bf16 v[168:171], v[40:43], v[84:87], v[168:171]\n"
    "ds_read_b64_tr_b16 v[84:85], v4 offset:8480\n"
    "s_waitcnt lgkmcnt(5)\n"
    "v_mfma_f32_16x16x32_bf16 v[48:51], v[192:195], v[56:59], 0\n"
    "ds_read_b64_tr_b16 v[86:87], v4 offset:8736\n"
    "v_cvt_pk_bf16_f32 v224, v160, v161\n"
    "v_mfma_f32_16x16x32_bf16 v[52:55], v[196:199], v[56:59], 0\n"
    "ds_read_b64_tr_b16 v[56:57], v7 offset:33344\n"
    "v_cvt_pk_bf16_f32 v225, v162, v163\n"
    "v_mfma_f32_16x16x32_bf16 v[92:95], v[196:199], v[60:63], 0\n"
    "ds_read_b64_tr_b16 v[58:59], v7 offset:33600\n"
    "v_cvt_pk_bf16_f32 v226, v164, v165\n"
    "v_mfma_f32_16x16x32_bf16 v[88:91], v[192:195], v[60:63], 0\n"
    "ds_read_b64_tr_b16 v[60:61], v7 offset:33376\n"
    "v_cvt_pk_bf16_f32 v227, v166, v167\n"
    "v_mfma_f32_16x16x32_bf16 v[48:51], v[200:203], v[64:67], v[48:51]\n"
    "ds_read_b64_tr_b16 v[62:63], v7 offset:33632\n"
    "v_cvt_pk_bf16_f32 v228, v168, v169\n"
    "v_mfma_f32_16x16x32_bf16 v[112:115], v[224:227], v[232:235], v[112:115]\n"
    "ds_read_b128 v[208:211], v2 offset:0\n"
    "v_cvt_pk_bf16_f32 v229, v170, v171\n"
    "v_mfma_f32_16x16x32_bf16 v[120:123], v[224:227], v[236:239], v[120:123]\n"
    "ds_read_b128 v[212:215], v2 offset:2048\n"
    "v_cvt_pk_bf16_f32 v230, v172, v173\n"
    "v_mfma_f32_16x16x32_bf16 v[52:55], v[204:207], v[64:67], v[52:55]\n"
    "ds_read_b64_tr_b16 v[64:65], v7 offset:41792\n"
    "v_cvt_pk_bf16_f32 v231, v174, v175\n"
    "v_mfma_f32_16x16x32_bf16 v[92:95], v[204:207], v[68:71], v[92:95]\n"
    "ds_read_b64_tr_b16 v[66:67], v7 offset:42048\n"
    "v_mfma_f32_16x16x32_bf16 v[116:119], v[228:231], v[232:235], v[116:119]\n"
    "ds_read_b128 v[216:219], v3 offset:0\n"
    "v_mfma_f32_16x16x32_bf16 v[124:127], v[228:231], v[236:239], v[124:127]\n"
    "ds_read_b128 v[220:223], v3 offset:2048\n"
    "v_mfma_f32_16x16x32_bf16 v[88:91], v[200:203], v[68:71], v[88:91]\n"
    "ds_read_b64_tr_b16 v[68:69], v7 offset:41824\n"
    "s_waitcnt lgkmcnt(5)\n"
    "v_mfma_f32_16x16x32_bf16 v[48:51], v[208:211], v[72:75], v[48:51]\n"
    "ds_read_b64_tr_b16 v[70:71], v7 offset:42080\n"
    "s_waitcnt vmcnt(0)\n"
    "v_mfma_f32_16x16x32_bf16 v[52:55], v[212:215], v[72:75], v[52:55]\n"
    "ds_read_b64_tr_b16 v[72:73], v4 offset:64\n"
    "v_max3_u32 v11, v248, v249, v250\n"
    "v_mfma_f32_16x16x32_bf16 v[92:95], v[212:215], v[76:79], v[92:95]\n"
    "ds_read_b64_tr_b16 v[74:75], v4 offset:320\n"
    "v_max3_u32 v11, v11, v251, v252\n"
    "v_mfma_f32_16x16x32_bf16 v[88:91], v[208:211], v[76:79], v[88:91]\n"
    "ds_read_b64_tr_b16 v[76:77], v4 offset:96\n"
    "s_waitcnt lgkmcnt(5)\n"
    "v_max3_u32 v11, v11, v253, v254\n"
    "v_mfma_f32_16x16x32_bf16 v[48:51], v[216:219], v[80:83], v[48:51]\n"
    "ds_read_b64_tr_b16 v[78:79], v4 offset:352\n"
    "v_max_u32 v11, v11, v255\n"
    "v_mfma_f32_16x16x32_bf16 v[52:55], v[220:223], v[80:83], v[52:55]\n"
    "ds_read_b64_tr_b16 v[80:81], v4 offset:8512\n"
    "v_cmp_eq_u32 vcc, -1, v11\n"
    "v_mfma_f32_16x16x32_bf16 v[92:95], v[220:223], v[84:87], v[92:95]\n"
    "ds_read_b64_tr_b16 v[82:83], v4 offset:8768\n"
    "v_mfma_f32_16x16x32_bf16 v[88:91], v[216:219], v[84:87], v[88:91]\n"
    "ds_read_b64_tr_b16 v[84:85], v4 offset:8544\n"
    "s_waitcnt lgkmcnt(7)\n"
    "s_cbranch_vccnz L_pf1_spin\n"
    "L_pf1_go:\n"
    "v_mfma_f32_16x16x32_bf16 v[160:163], v[192:195], v[56:59], 0\n"
    "ds_read_b64_tr_b16 v[86:87], v4 offset:8800\n"
    "v_cvt_pk_bf16_f32 v224, v48, v49\n"
    "v_mfma_f32_16x16x32_bf16 v[164:167], v[196:199], v[56:59], 0\n"
    "ds_read_b64_tr_b16 v[56:57], v4 offset:33408\n"
    "v_cvt_pk_bf16_f32 v225, v50, v51\n"
    "v_mfma_f32_16x16x32_bf16 v[172:175], v[196:199], v[60:63], 0\n"
    "ds_read_b64_tr_b16 v[58:59], v4 offset:33664\n"
    "v_cvt_pk_bf16_f32 v226, v52, v53\n"
    "v_mfma_f32_16x16x32_bf16 v[168:171], v[192:195], v[60:63], 0\n"
    "ds_read_b64_tr_b16 v[60:61], v7 offset:33408\n"
    "v_cvt_pk_bf16_f32 v227, v54, v55\n"
    "v_mfma_f32_16x16x32_bf16 v[160:163], v[200:203], v[64:67], v[160:163]\n"
    "ds_read_b64_tr_b16 v[62:63], v7 offset:33664\n"
    "v_cvt_pk_bf16_f32 v228, v88, v89\n"
    "v_mfma_f32_16x16x32_bf16 v[96:99], v[224:227], v[248:251], v[96:99]\n"
    "v_cvt_pk_bf16_f32 v229, v90, v91\n"
    "v_mfma_f32_16x16x32_bf16 v[104:107], v[224:227], v[252:255], v[104:107]\n"
    "v_cvt_pk_bf16_f32 v230, v92, v93\n"
    "v_mfma_f32_16x16x32_bf16 v[164:167], v[204:207], v[64:67], v[164:167]\n"
    "ds_read_b64_tr_b16 v[64:65], v4 offset:41856\n"
    "v_cvt_pk_bf16_f32 v231, v94, v95\n"
    "v_mfma_f32_16x16x32_bf16 v[172:175], v[204:207], v[68:71], v[172:175]\n"
    "ds_read_b64_tr_b16 v[66:67], v4 offset:42112\n"
    "v_mfma_f32_16x16x32_bf16 v[100:103], v[228:231], v[248:251], v[100:103]\n"
    "v_cvt_pk_bf16_f32 v176, v96, v97\n"
    "v_cvt_pk_bf16_f32 v177, v98, v99\n"
    "v_mfma_f32_16x16x32_bf16 v[108:111], v[228:231], v[252:255], v[108:111]\n"
    "v_cvt_pk_bf16_f32 v178, v104, v105\n"
    "v_cvt_pk_bf16_f32 v179, v106, v107\n"
    "v_mfma_f32_16x16x32_bf16 v[168:171], v[200:203], v[68:71], v[168:171]\n"
    "ds_read_b64_tr_b16 v[68:69], v7 offset:41856\n"
    "s_waitcnt lgkmcnt(7)\n"
    "v_mfma_f32_16x16x32_bf16 v[160:163], v[208:211], v[72:75], v[160:163]\n"
    "ds_read_b64_tr_b16 v[70:71], v7 offset:42112\n"
    "v_min_u32 v176, -2, v176\n"
    "v_min_u32 v177, -2, v177\n"
    "v_mfma_f32_16x16x32_bf16 v[164:167], v[212:215], v[72:75], v[164:167]\n"
    "ds_read_b64_tr_b16 v[72:73], v7 offset:128\n"
    "v_min_u32 v178, -2, v178\n"
    "v_min_u32 v179, -2, v179\n"
    "v_mfma_f32_16x16x32_bf16 v[172:175], v[212:215], v[76:79], v[172:175]\n"
    "ds_read_b64_tr_b16 v[74:75], v7 offset:384\n"
    "buffer_store_dwordx4 v[176:179], v8, s[36:39], s54 offen offset:0 sc1\n"
    "v_mfma_f32_16x16x32_bf16 v[168:171], v[208:211], v[76:79], v[168:171]\n"
    "ds_read_b64_tr_b16 v[76:77], v4 offset:128\n"
    "v_cvt_pk_bf16_f32 v180, v100, v101\n"
    "v_cvt_pk_bf16_f32 v181, v102, v103\n"
    "v_mfma_f32_16x16x32_bf16 v[160:163], v[216:219], v[80:83], v[160:163]\n"
    "ds_read_b64_tr_b16 v[78:79], v4 offset:384\n"
    "v_cvt_pk_bf16_f32 v182, v108, v109\n"
    "v_cvt_pk_bf16_f32 v183, v110, v111\n"
    "v_mfma_f32_16x16x32_bf16 v[164:167], v[220:223], v[80:83], v[164:167]\n"
    "ds_read_b64_tr_b16 v[80:81], v7 offset:8576\n"
    "v_min_u32 v180, -2, v180\n"
    "v_min_u32 v181, -2, v181\n"
    "v_mfma_f32_16x16x32_bf16 v[172:175], v[220:223], v[84:87], v[172:175]\n"
    "ds_read_b64_tr_b16 v[82:83], v7 offset:8832\n"
    "v_min_u32 v182, -2, v182\n"
    "v_min_u32 v183, -2, v183\n"
    "v_mfma_f32_16x16x32_bf16 v[168:171], v[216:219], v[84:87], v[168:171]\n"
    "ds_read_b64_tr_b16 v[84:85], v4 offset:8576\n"
    "s_waitcnt lgkmcnt(7)\n"
    "v_mfma_f32_16x16x32_bf16 v[48:51], v[16:19], v[56:59], 0\n"
    "ds_read_b64_tr_b16 v[86:87], v4 offset:8832\n"
    "v_cvt_pk_bf16_f32 v224, v160, v161\n"
    "v_mfma_f32_16x16x32_bf16 v[52:55], v[20:23], v[56:59], 0\n"
    "ds_read_b64_tr_b16 v[56:57], v4 offset:33440\n"
    "v_cvt_pk_bf16_f32 v225, v162, v163\n"
    "v_mfma_f32_16x16x32_bf16 v[92:95], v[196:199], v[60:63], 0\n"
    "ds_read_b64_tr_b16 v[58:59], v4 offset:33696\n"
    "v_cvt_pk_bf16_f32 v226, v164, v165\n"
    "v_mfma_f32_16x16x32_bf16 v[88:91], v[192:195], v[60:63], 0\n"
    "ds_read_b64_tr_b16 v[60:61], v7 offset:33440\n"
    "v_cvt_pk_bf16_f32 v227, v166, v167\n"
    "v_mfma_f32_16x16x32_bf16 v[48:51], v[24:27], v[64:67], v[48:51]\n"
    "ds_read_b64_tr_b16 v[62:63], v7 offset:33696\n"
    "v_cvt_pk_bf16_f32 v228, v168, v169\n"
    "v_mfma_f32_16x16x32_bf16 v[112:115], v[224:227], v[248:251], v[112:115]\n"
    "v_cvt_pk_bf16_f32 v229, v170, v171\n"
    "v_mfma_f32_16x16x32_bf16 v[120:123], v[224:227], v[252:255], v[120:123]\n"
    "v_cvt_pk_bf16_f32 v230, v172, v173\n"
    "buffer_store_dwordx4 v[180:183], v8, s[36:39], s54 offen offset:1024 sc1\n"
    "v_mfma_f32_16x16x32_bf16 v[52:55], v[28:31], v[64:67], v[52:55]\n"
    "ds_read_b64_tr_b16 v[64:65], v4 offset:41888\n"
    "v_cvt_pk_bf16_f32 v231, v174, v175\n"
    "v_mfma_f32_16x16x32_bf16 v[92:95], v[204:207], v[68:71], v[92:95]\n"
    "ds_read_b64_tr_b16 v[66:67], v4 offset:42144\n"
    "v_mfma_f32_16x16x32_bf16 v[116:119], v[228:231], v[248:251], v[116:119]\n"
    "v_cvt_pk_bf16_f32 v176, v112, v113\n"
    "v_cvt_pk_bf16_f32 v177, v114, v115\n"
    "v_mfma_f32_16x16x32_bf16 v[124:127], v[228:231], v[252:255], v[124:127]\n"
    "v_cvt_pk_bf16_f32 v178, v120, v121\n"
    "v_cvt_pk_bf16_f32 v179, v122, v123\n"
    "v_mfma_f32_16x16x32_bf16 v[88:91], v[200:203], v[68:71], v[88:91]\n"
    "ds_read_b64_tr_b16 v[68:69], v7 offset:41888\n"
    "s_waitcnt lgkmcnt(7)\n"
    "v_mfma_f32_16x16x32_bf16 v[48:51], v[32:35], v[72:75], v[48:51]\n"
    "ds_read_b64_tr_b16 v[70:71], v7 offset:42144\n"
    "v_min_u32 v176, -2, v176\n"
    "v_mfma_f32_16x16x32_bf16 v[52:55], v[36:39], v[72:75], v[52:55]\n"
    "ds_read_b64_tr_b16 v[72:73], v7 offset:160\n"
    "v_min_u32 v177, -2, v177\n"
    "v_mfma_f32_16x16x32_bf16 v[92:95], v[212:215], v[76:79], v[92:95]\n"
    "ds_read_b64_tr_b16 v[74:75], v7 offset:416\n"
    "v_min_u32 v178, -2, v178\n"
    "v_mfma_f32_16x16x32_bf16 v[88:91], v[208:211], v[76:79], v[88:91]\n"
    "ds_read_b64_tr_b16 v[76:77], v4 offset:160\n"
    "v_min_u32 v179, -2, v179\n"
    "v_mfma_f32_16x16x32_bf16 v[48:51], v[40:43], v[80:83], v[48:51]\n"
    "ds_read_b64_tr_b16 v[78:79], v4 offset:416\n"
    "v_cvt_pk_bf16_f32 v180, v116, v117\n"
    "v_mfma_f32_16x16x32_bf16 v[52:55], v[44:47], v[80:83], v[52:55]\n"
    "ds_read_b64_tr_b16 v[80:81], v7 offset:8608\n"
    "v_cvt_pk_bf16_f32 v181, v118, v119\n"
    "v_mfma_f32_16x16x32_bf16 v[92:95], v[220:223], v[84:87], v[92:95]\n"
    "ds_read_b64_tr_b16 v[82:83], v7 offset:8864\n"
    "buffer_store_dwordx4 v[176:179], v8, s[36:39], s54 offen offset:2048 sc1\n"
    "v_mfma_f32_16x16x32_bf16 v[88:91], v[216:219], v[84:87], v[88:91]\n"
    "ds_read_b64_tr_b16 v[84:85], v4 offset:8608\n"
    "s_waitcnt lgkmcnt(7)\n"
    "v_mfma_f32_16x16x32_bf16 v[160:163], v[16:19], v[56:59], 0\n"
    "ds_read_b64_tr_b16 v[86:87], v4 offset:8864\n"
    "v_cvt_pk_bf16_f32 v224, v48, v49\n"
    "v_mfma_f32_16x16x32_bf16 v[164:167], v[20:23], v[56:59], 0\n"
    "ds_read_b64_tr_b16 v[56:57], v4 offset:33472\n"
    "v_cvt_pk_bf16_f32 v225, v50, v51\n"
    "v_mfma_f32_16x16x32_bf16 v[172:175], v[196:199], v[60:63], 0\n"
    "ds_read_b64_tr_b16 v[58:59], v4 offset:33728\n"
    "v_cvt_pk_bf16_f32 v226, v52, v53\n"
    "v_mfma_f32_16x16x32_bf16 v[168:171], v[192:195], v[60:63], 0\n"
    "ds_read_b64_tr_b16 v[60:61], v7 offset:33472\n"
    "v_cvt_pk_bf16_f32 v227, v54, v55\n"
    "v_mfma_f32_16x16x32_bf16 v[160:163], v[24:27], v[64:67], v[160:163]\n"
    "ds_read_b64_tr_b16 v[62:63], v7 offset:33728\n"
    "v_cvt_pk_bf16_f32 v228, v88, v89\n"
    "v_mfma_f32_16x16x32_bf16 v[128:131], v[224:227], v[232:235], v[128:131]\n"
    "v_cvt_pk_bf16_f32 v229, v90, v91\n"
    "v_mfma_f32_16x16x32_bf16 v[136:139], v[224:227], v[236:239], v[136:139]\n"
    "v_cvt_pk_bf16_f32 v230, v92, v93\n"
    "v_mfma_f32_16x16x32_bf16 v[164:167], v[28:31], v[64:67], v[164:167]\n"
    "ds_read_b64_tr_b16 v[64:65], v4 offset:41920\n"
    "v_cvt_pk_bf16_f32 v231, v94, v95\n"
    "v_mfma_f32_16x16x32_bf16 v[172:175], v[204:207], v[68:71], v[172:175]\n"
    "ds_read_b64_tr_b16 v[66:67], v4 offset:42176\n"
    "v_cvt_pk_bf16_f32 v182, v124, v125\n"
    "v_mfma_f32_16x16x32_bf16 v[128:131], v[228:231], v[248:251], v[128:131]\n"
    "v_cvt_pk_bf16_f32 v183, v126, v127\n"
    "v_min_u32 v180, -2, v180\n"
    "v_mfma_f32_16x16x32_bf16 v[136:139], v[228:231], v[252:255], v[136:139]\n"
    "v_min_u32 v181, -2, v181\n"
    "v_min_u32 v182, -2, v182\n"
    "v_mfma_f32_16x16x32_bf16 v[168:171], v[200:203], v[68:71], v[168:171]\n"
    "ds_read_b64_tr_b16 v[68:69], v7 offset:41920\n"
    "s_waitcnt lgkmcnt(7)\n"
    "v_mfma_f32_16x16x32_bf16 v[160:163], v[32:35], v[72:75], v[160:163]\n"
    "ds_read_b64_tr_b16 v[70:71], v7 offset:42176\n"
    "v_min_u32 v183, -2, v183\n"
    "buffer_store_dwordx4 v[180:183], v8, s[36:39], s54 offen offset:3072 sc1\n"
    "v_mfma_f32_16x16x32_bf16 v[164:167], v[36:39], v[72:75], v[164:167]\n"
    "ds_read_b64_tr_b16 v[72:73], v7 offset:192\n"
    "v_cvt_pk_bf16_f32 v176, v128, v129\n"
    "v_mfma_f32_16x16x32_bf16 v[172:175], v[212:215], v[76:79], v[172:175]\n"
    "ds_read_b64_tr_b16 v[74:75], v7 offset:448\n"
    "v_cvt_pk_bf16_f32 v177, v130, v131\n"
    "v_mfma_f32_16x16x32_bf16 v[168:171], v[208:211], v[76:79], v[168:171]\n"
    "ds_read_b64_tr_b16 v[76:77], v4 offset:192\n"
    "v_cvt_pk_bf16_f32 v178, v136, v137\n"
    "v_mfma_f32_16x16x32_bf16 v[160:163], v[40:43], v[80:83], v[160:163]\n"
    "ds_read_b64_tr_b16 v[78:79], v4 offset:448\n"
    "v_cvt_pk_bf16_f32 v179, v138, v139\n"
    "v_mfma_f32_16x16x32_bf16 v[164:167], v[44:47], v[80:83], v[164:167]\n"
    "ds_read_b64_tr_b16 v[80:81], v7 offset:8640\n"
    "v_min_u32 v176, -2, v176\n"
    "v_min_u32 v177, -2, v177\n"
    "v_mfma_f32_16x16x32_bf16 v[172:175], v[220:223], v[84:87], v[172:175]\n"
    "ds_read_b64_tr_b16 v[82:83], v7 offset:8896\n"
    "v_min_u32 v178, -2, v178\n"
    "v_min_u32 v179, -2, v179\n"
    "v_mfma_f32_16x16x32_bf16 v[168:171], v[216:219], v[84:87], v[168:171]\n"
    "ds_read_b64_tr_b16 v[84:85], v4 offset:8640\n"
    "s_waitcnt lgkmcnt(7)\n"
    "v_mfma_f32_16x16x32_bf16 v[48:51], v[16:19], v[56:59], 0\n"
    "ds_read_b64_tr_b16 v[86:87], v4 offset:8896\n"
    "v_cvt_pk_bf16_f32 v224, v160, v161\n"
    "v_mfma_f32_16x16x32_bf16 v[52:55], v[20:23], v[56:59], 0\n"
    "ds_read_b64_tr_b16 v[56:57], v4 offset:33504\n"
    "v_cvt_pk_bf16_f32 v225, v162, v163\n"
    "v_mfma_f32_16x16x32_bf16 v[92:95], v[196:199], v[60:63], 0\n"
    "ds_read_b64_tr_b16 v[58:59], v4 offset:33760\n"
    "v_cvt_pk_bf16_f32 v226, v164, v165\n"
    "v_mfma_f32_16x16x32_bf16 v[88:91], v[192:195], v[60:63], 0\n"
    "ds_read_b64_tr_b16 v[60:61], v7 offset:33504\n"
    "v_cvt_pk_bf16_f32 v227, v166, v167\n"
    "v_mfma_f32_16x16x32_bf16 v[48:51], v[24:27], v[64:67], v[48:51]\n"
    "ds_read_b64_tr_b16 v[62:63], v7 offset:33760\n"
    "v_cvt_pk_bf16_f32 v228, v168, v169\n"
    "v_mfma_f32_16x16x32_bf16 v[132:135], v[224:227], v[232:235], v[132:135]\n"
    "v_cvt_pk_bf16_f32 v229, v170, v171\n"
    "buffer_store_dwordx4 v[176:179], v8, s[36:39], s55 offen offset:0 sc1\n"
    "v_mfma_f32_16x16x32_bf16 v[140:143], v[224:227], v[236:239], v[140:143]\n"
    "v_cvt_pk_bf16_f32 v230, v172, v173\n"
    "v_mfma_f32_16x16x32_bf16 v[52:55], v[28:31], v[64:67], v[52:55]\n"
    "ds_read_b64_tr_b16 v[64:65], v4 offset:41952\n"
    "v_cvt_pk_bf16_f32 v231, v174, v175\n"
    "v_mfma_f32_16x16x32_bf16 v[92:95], v[204:207], v[68:71], v[92:95]\n"
    "ds_read_b64_tr_b16 v[66:67], v4 offset:42208\n"
    "v_mfma_f32_16x16x32_bf16 v[132:135], v[228:231], v[248:251], v[132:135]\n"
    "v_mfma_f32_16x16x32_bf16 v[140:143], v[228:231], v[252:255], v[140:143]\n"
    "v_mfma_f32_16x16x32_bf16 v[88:91], v[200:203], v[68:71], v[88:91]\n"
    "ds_read_b64_tr_b16 v[68:69], v7 offset:41952\n"
    "s_waitcnt lgkmcnt(7)\n"
    "v_mfma_f32_16x16x32_bf16 v[48:51], v[32:35], v[72:75], v[48:51]\n"
    "ds_read_b64_tr_b16 v[70:71], v7 offset:42208\n"
    "v_mfma_f32_16x16x32_bf16 v[52:55], v[36:39], v[72:75], v[52:55]\n"
    "ds_read_b64_tr_b16 v[72:73], v7 offset:224\n"
    "v_cvt_pk_bf16_f32 v180, v132, v133\n"
    "v_cvt_pk_bf16_f32 v181, v134, v135\n"
    "v_mfma_f32_16x16x32_bf16 v[92:95], v[212:215], v[76:79], v[92:95]\n"
    "ds_read_b64_tr_b16 v[74:75], v7 offset:480\n"
    "v_cvt_pk_bf16_f32 v182, v140, v141\n"
    "v_cvt_pk_bf16_f32 v183, v142, v143\n"
    "v_mfma_f32_16x16x32_bf16 v[88:91], v[208:211], v[76:79], v[88:91]\n"
    "ds_read_b64_tr_b16 v[76:77], v4 offset:224\n"
    "v_min_u32 v180, -2, v180\n"
    "v_min_u32 v181, -2, v181\n"
    "v_mfma_f32_16x16x32_bf16 v[48:51], v[40:43], v[80:83], v[48:51]\n"
    "ds_read_b64_tr_b16 v[78:79], v4 offset:480\n"
    "v_min_u32 v182, -2, v182\n"
    "v_min_u32 v183, -2, v183\n"
    "v_mfma_f32_16x16x32_bf16 v[52:55], v[44:47], v[80:83], v[52:55]\n"
    "ds_read_b64_tr_b16 v[80:81], v7 offset:8672\n"
    "buffer_store_dwordx4 v[180:183], v8, s[36:39], s55 offen offset:1024 sc1\n"
    "v_mfma_f32_16x16x32_bf16 v[92:95], v[220:223], v[84:87], v[92:95]\n"
    "ds_read_b64_tr_b16 v[82:83], v7 offset:8928\n"
    "v_mfma_f32_16x16x32_bf16 v[88:91], v[216:219], v[84:87], v[88:91]\n"
    "ds_read_b64_tr_b16 v[84:85], v4 offset:8672\n"
    "s_waitcnt lgkmcnt(7)\n"
    "v_mfma_f32_16x16x32_bf16 v[160:163], v[16:19], v[56:59], 0\n"
    "ds_read_b64_tr_b16 v[86:87], v4 offset:8928\n"
    "v_cvt_pk_bf16_f32 v224, v48, v49\n"
    "v_mfma_f32_16x16x32_bf16 v[164:167], v[20:23], v[56:59], 0\n"
    "v_cvt_pk_bf16_f32 v225, v50, v51\n"
    "v_mfma_f32_16x16x32_bf16 v[172:175], v[196:199], v[60:63], 0\n"
    "v_cvt_pk_bf16_f32 v226, v52, v53\n"
    "v_mfma_f32_16x16x32_bf16 v[168:171], v[192:195], v[60:63], 0\n"
    "v_cvt_pk_bf16_f32 v227, v54, v55\n"
    "v_mfma_f32_16x16x32_bf16 v[160:163], v[24:27], v[64:67], v[160:163]\n"
    "v_cvt_pk_bf16_f32 v228, v88, v89\n"
    "v_mfma_f32_16x16x32_bf16 v[144:147], v[224:227], v[232:235], v[144:147]\n"
    "v_cvt_pk_bf16_f32 v229, v90, v91\n"
    "v_mfma_f32_16x16x32_bf16 v[152:155], v[224:227], v[236:239], v[152:155]\n"
    "v_cvt_pk_bf16_f32 v230, v92, v93\n"
    "v_mfma_f32_16x16x32_bf16 v[164:167], v[28:31], v[64:67], v[164:167]\n"
    "v_cvt_pk_bf16_f32 v231, v94, v95\n"
    "v_mfma_f32_16x16x32_bf16 v[172:175], v[204:207], v[68:71], v[172:175]\n"
    "s_nop 0\n"
    "v_mfma_f32_16x16x32_bf16 v[144:147], v[228:231], v[248:251], v[144:147]\n"
    "v_mfma_f32_16x16x32_bf16 v[152:155], v[228:231], v[252:255], v[152:155]\n"
    "v_mfma_f32_16x16x32_bf16 v[168:171], v[200:203], v[68:71], v[168:171]\n"
    "s_waitcnt lgkmcnt(0)\n"
    "v_mfma_f32_16x16x32_bf16 v[160:163], v[32:35], v[72:75], v[160:163]\n"
    "v_mfma_f32_16x16x32_bf16 v[164:167], v[36:39], v[72:75], v[164:167]\n"
    "v_mfma_f32_16x16x32_bf16 v[172:175], v[212:215], v[76:79], v[172:175]\n"
    "v_mfma_f32_16x16x32_bf16 v[168:171], v[208:211], v[76:79], v[168:171]\n"
    "v_mfma_f32_16x16x32_bf16 v[160:163], v[40:43], v[80:83], v[160:163]\n"
    "ds_write_b128 v12, v[144:147] offset:33280\n"
    "v_mfma_f32_16x16x32_bf16 v[164:167], v[44:47], v[80:83], v[164:167]\n"
    "v_mfma_f32_16x16x32_bf16 v[172:175], v[220:223], v[84:87], v[172:175]\n"
    "ds_write_b128 v12, v[152:155] offset:35328\n"
    "v_mfma_f32_16x16x32_bf16 v[168:171], v[216:219], v[84:87], v[168:171]\n"
    "s_nop 4\n"
    "v_cvt_pk_bf16_f32 v224, v160, v161\n"
    "v_cvt_pk_bf16_f32 v225, v162, v163\n"
    "v_cvt_pk_bf16_f32 v226, v164, v165\n"
    "v_cvt_pk_bf16_f32 v227, v166, v167\n"
    "v_cvt_pk_bf16_f32 v228, v168, v169\n"
    "v_cvt_pk_bf16_f32 v229, v170, v171\n"
    "v_cvt_pk_bf16_f32 v230, v172, v173\n"
    "v_cvt_pk_bf16_f32 v231, v174, v175\n"
    "v_mfma_f32_16x16x32_bf16 v[148:151], v[224:227], v[232:235], v[148:151]\n"
    "v_mfma_f32_16x16x32_bf16 v[156:159], v[224:227], v[236:239], v[156:159]\n"
    "v_mfma_f32_16x16x32_bf16 v[148:151], v[228:231], v[248:251], v[148:151]\n"
    "v_mfma_f32_16x16x32_bf16 v[156:159], v[228:231], v[252:255], v[156:159]\n"
    "s_nop 4\n"
    "s_nop 1\n"
    "ds_write_b128 v12, v[148:151] offset:34304\n"
    "ds_write_b128 v12, v[156:159] offset:36352\n"
    "s_waitcnt lgkmcnt(0)\n"
    "s_barrier\n"
    "s_endpgm\n"
    "L_pa_spin:\n"
    "buffer_load_dwordx4 v[232:235], v8, s[36:39], s48 offen offset:0 sc1\n"
    "buffer_load_dwordx4 v[236:239], v8, s[36:39], s48 offen offset:1024 sc1\n"
    "s_waitcnt vmcnt(0)\n"
    "v_max3_u32 v11, v232, v233, v234\n"
    "v_max3_u32 v11, v11, v235, v236\n"
    "v_max3_u32 v11, v11, v237, v238\n"
    "v_max_u32 v11, v11, v239\n"
    "v_cmp_eq_u32 vcc, -1, v11\n"
    "s_cbranch_vccz L_pa_go\n"
    "s_sub_u32 s15, s15, 1\n"
    "s_cbranch_scc1 L_exit\n"
    "s_branch L_pa_spin\n"
    "L_pb_spin:\n"
    "buffer_load_dwordx4 v[240:243], v8, s[36:39], s48 offen offset:0 sc1\n"
    "buffer_load_dwordx4 v[244:247], v8, s[36:39], s48 offen offset:1024 sc1\n"
    "s_waitcnt vmcnt(0)\n"
    "v_max3_u32 v11, v240, v241, v242\n"
    "v_max3_u32 v11, v11, v243, v244\n"
    "v_max3_u32 v11, v11, v245, v246\n"
    "v_max_u32 v11, v11, v247\n"
    "v_cmp_eq_u32 vcc, -1, v11\n"
    "s_cbranch_vccz L_pb_go\n"
    "s_sub_u32 s15, s15, 1\n"
    "s_cbranch_scc1 L_exit\n"
    "s_branch L_pb_spin\n"
    "L_pf0_spin:\n"
    "buffer_load_dwordx4 v[232:235], v8, s[36:39], s50 offen offset:0 sc1\n"
    "buffer_load_dwordx4 v[236:239], v8, s[36:39], s50 offen offset:1024 sc1\n"
    "s_waitcnt vmcnt(0)\n"
    "v_max3_u32 v11, v232, v233, v234\n"
    "v_max3_u32 v11, v11, v235, v236\n"
    "v_max3_u32 v11, v11, v237, v238\n"
    "v_max_u32 v11, v11, v239\n"
    "v_cmp_eq_u32 vcc, -1, v11\n"
    "s_cbranch_vccz L_pf0_go\n"
    "s_sub_u32 s15, s15, 1\n"
    "s_cbranch_scc1 L_exit\n"
    "s_branch L_pf0_spin\n"
    "L_pf1_spin:\n"
    "buffer_load_dwordx4 v[248:251], v8, s[36:39], s48 offen offset:0 sc1\n"
    "buffer_load_dwordx4 v[252:255], v8, s[36:39], s48 offen offset:1024 sc1\n"
    "s_waitcnt vmcnt(0)\n"
    "v_max3_u32 v11, v248, v249, v250\n"
    "v_max3_u32 v11, v11, v251, v252\n"
    "v_max3_u32 v11, v11, v253, v254\n"
    "v_max_u32 v11, v11, v255\n"
    "v_cmp_eq_u32 vcc, -1, v11\n"
    "s_cbranch_vccz L_pf1_go\n"
    "s_sub_u32 s15, s15, 1\n"
    "s_cbranch_scc1 L_exit\n"
    "s_branch L_pf1_spin\n"
    "L_exit:\n"
    "s_endpgm\n"
    :
    : "s"(__builtin_amdgcn_kernarg_segment_ptr()), "s"(blockIdx.x), "{v0}"(tid)
    : "v1", "v2", "v3", "v4", "v5", "v6", "v7", "v8", "v9", "v10", "v11", "v12", "v13", "v14", "v15", "v16", "v17", "v18", "v19", "v20", "v21", "v22", "v23", "v24", "v25", "v26", "v27", "v28", "v29", "v30", "v31", "v32", "v33", "v34", "v35", "v36", "v37", "v38", "v39", "v40", "v41", "v42", "v43", "v44", "v45", "v46", "v47", "v48", "v49", "v50", "v51", "v52", "v53", "v54", "v55", "v56", "v57", "v58", "v59", "v60", "v61", "v62", "v63", "v64", "v65", "v66", "v67", "v68", "v69", "v70", "v71", "v72", "v73", "v74", "v75", "v76", "v77", "v78", "v79", "v80", "v81", "v82", "v83", "v84", "v85", "v86", "v87", "v88", "v89", "v90", "v91", "v92", "v93", "v94", "v95", "v96", "v97", "v98", "v99", "v100", "v101", "v102", "v103", "v104", "v105", "v106", "v107", "v108", "v109", "v110", "v111", "v112", "v113", "v114", "v115", "v116", "v117", "v118", "v119", "v120", "v121", "v122", "v123", "v124", "v125", "v126", "v127", "v128", "v129", "v130", "v131", "v132", "v133", "v134", "v135", "v136", "v137", "v138", "v139", "v140", "v141", "v142", "v143", "v144", "v145", "v146", "v147", "v148", "v149", "v150", "v151", "v152", "v153", "v154", "v155", "v156", "v157", "v158", "v159", "v160", "v161", "v162", "v163", "v164", "v165", "v166", "v167", "v168", "v169", "v170", "v171", "v172", "v173", "v174", "v175", "v176", "v177", "v178", "v179", "v180", "v181", "v182", "v183", "v184", "v185", "v186", "v187", "v188", "v189", "v190", "v191", "v192", "v193", "v194", "v195", "v196", "v197", "v198", "v199", "v200", "v201", "v202", "v203", "v204", "v205", "v206", "v207", "v208", "v209", "v210", "v211", "v212", "v213", "v214", "v215", "v216", "v217", "v218", "v219", "v220", "v221", "v222", "v223", "v224", "v225", "v226", "v227", "v228", "v229", "v230", "v231", "v232", "v233", "v234", "v235", "v236", "v237", "v238", "v239", "v240", "v241", "v242", "v243", "v244", "v245", "v246", "v247", "v248", "v249", "v250", "v251", "v252", "v253", "v254", "v255", "s4", "s5", "s6", "s7", "s8", "s9", "s10", "s11", "s12", "s13", "s14", "s15", "s16", "s17", "s18", "s19", "s20", "s21", "s22", "s23", "s24", "s25", "s26", "s27", "s28", "s29", "s30", "s31", "s33", "s34", "s35", "s36", "s37", "s38", "s39", "s40", "s41", "s42", "s43", "s44", "s45", "s46", "s47", "s48", "s49", "s50", "s51", "s52", "s53", "s54", "s55", "s56", "s57", "s58", "s59", "s60", "s61", "s62", "s63", "s64", "s65", "s66", "s67", "s68", "s69", "s70", "s71", "s72", "s73", "s74", "s75", "s76", "s77", "s78", "s79", "s80", "s81", "s82", "s83", "s84", "s85", "s86", "s87", "s88", "s89", "s90", "s91", "s92", "s93", "s94", "s95", "vcc", "scc", "memory");
}

extern "C" void kernel_launch(void* const* d_in, const int* in_sizes, int n_in, void* d_out, int out_size, void* d_ws,
                              size_t ws_size, hipStream_t stream) {
  Params p;
  for (int br = 0; br < 2; ++br) {
    p.b[br].x = d_in[5 * br]; p.b[br].k = d_in[5 * br + 1]; p.b[br].v = d_in[5 * br + 2];
    p.b[br].wq = d_in[5 * br + 3]; p.b[br].wo = d_in[5 * br + 4];
    p.b[br].out = d_out; p.b[br].ws = d_ws; p.b[br].ws2 = d_ws;
  }
  (void)hipFuncSetAttribute((const void*)mha2_fused, hipFuncAttributeMaxDynamicSharedMemorySize, 133120);
  mha2_fused<<<256, 512, 133120, stream>>>(p);
}
```
